```python
import torch

def _softmax(x, axis=-1):
    return torch.softmax(x, dim=axis)
def _gelu(x):
    return torch.nn.functional.gelu(x, approximate="tanh")
def _silu(x):
    return torch.nn.functional.silu(x)
def _sigmoid(x):
    return torch.sigmoid(x)
def _layernorm(x, axis=-1, eps=1e-5):
    m = x.mean(dim=axis, keepdim=True)
    v = x.var(dim=axis, keepdim=True, unbiased=False)
    return (x - m) / torch.sqrt(v + eps)
def _scatter_add(tgt, idx, src, axis=0):
    return tgt.clone().index_add_(axis, idx, src)
def _segment_sum(src, seg, n):
    out = torch.zeros((n,) + src.shape[1:], dtype=src.dtype)
    return out.index_add_(0, seg, src)
def _pad(x, pw):
    flat = tuple(v for pair in reversed(pw) for v in pair)
    return torch.nn.functional.pad(x, flat)

kernel_name = 'recurrent_scan__linear_kernel_hedgehog__mixer_token_mlp__prompt'

def setup_inputs():

    _w = (lambda a: (lambda t: t.to(torch.bfloat16) if t.is_floating_point() else t)(torch.as_tensor(a)))
    def _A_setup():
        B, S, D, H = 1, 2048, 512, 32
        Dh = D // H
        return {
            "x": _w((lambda *s: torch.randn(*s, dtype=torch.bfloat16) * 0.05)(B, S, D)),
            "wqkv": _w((lambda *s: torch.randn(*s, dtype=torch.bfloat16) * 0.4)(D, 3 * D)),
            "w_phi": _w((lambda *s: torch.randn(*s, dtype=torch.bfloat16) * 0.4)(H, Dh, Dh) * (Dh ** -0.5)),
            "wo": _w((lambda *s: torch.randn(*s, dtype=torch.bfloat16) * 0.2)(D, D)),
        }
    _A_in = _A_setup()

    def _B_setup():
        B, S, D, F = 1, 2048, 512, 1376
        return {
            "x": _w((lambda *s: torch.randn(*s, dtype=torch.bfloat16) * 0.05)(B, S, D)),
            "w1": _w((lambda *s: torch.randn(*s, dtype=torch.bfloat16) * 0.4)(S, F) * (S ** -0.5)),
            "b1": _w((lambda *s: torch.randn(*s, dtype=torch.bfloat16) * 3.2)(F) * 0.02),
            "w2": _w((lambda *s: torch.randn(*s, dtype=torch.bfloat16) * 0.4)(F, S) * (F ** -0.5)),
            "b2": _w((lambda *s: torch.randn(*s, dtype=torch.bfloat16) * 0.8)(S) * 0.02),
        }
    _B_in = _B_setup()

    return {
        "A_x": _A_in["x"],
        "A_wqkv": _A_in["wqkv"],
        "A_w_phi": _A_in["w_phi"],
        "A_wo": _A_in["wo"],
        "B_x": _B_in["x"],
        "B_w1": _B_in["w1"],
        "B_b1": _B_in["b1"],
        "B_w2": _B_in["w2"],
        "B_b2": _B_in["b2"],
    }

def reference(A_x, A_wqkv, A_w_phi, A_wo, B_x, B_w1, B_b1, B_w2, B_b2):
    def _A_ref(x, wqkv, w_phi, wo):
        B, S, D = x.shape
        H = 32; Dh = D // H
        qkv = (lambda x, *d: x.permute(*d))((x @ wqkv).reshape(B, S, 3, H, Dh), 2, 0, 3, 1, 4)
        q, k, v = qkv[0], qkv[1], qkv[2]
        pq = torch.einsum("bhsd,hdf->bhsf", q, w_phi)
        pk = torch.einsum("bhsd,hdf->bhsf", k, w_phi)
        phi_q = torch.concatenate([_softmax(pq, axis=-1), _softmax(-pq, axis=-1)], axis=-1)
        phi_k = torch.concatenate([_softmax(pk, axis=-1), _softmax(-pk, axis=-1)], axis=-1)
        kv = torch.einsum("bhsf,bhsd->bhfd", phi_k, v)
        z  = torch.einsum("bhsf->bhf", phi_k)
        num = torch.einsum("bhqf,bhfd->bhqd", phi_q, kv)
        den = torch.einsum("bhqf,bhf->bhq", phi_q, z)[..., None] + 1e-6
        o = num / den
        return (lambda x, *d: x.permute(*d))(o, 0, 2, 1, 3).reshape(B, S, D) @ wo

    def _B_ref(x, w1, b1, w2, b2):
        B, S, D = x.shape
        h = _layernorm(x, axis=-1)
        u = torch.einsum("bsd,sf->bdf", h, w1) + b1
        u = _gelu(u)
        y = torch.einsum("bdf,fs->bds", u, w2) + b2
        return (lambda x, *d: x.permute(*d))(y, 0, 2, 1)

    x = A_x
    xn = _layernorm(x, axis=-1)
    s = _A_ref(xn, A_wqkv, A_w_phi, A_wo)
    g = _silu(_B_ref(xn, B_w1, B_b1, B_w2, B_b2))
    return x + s * g
```

```cpp
#include <hip/hip_runtime.h>
#include <hip/hip_bf16.h>
#include <stdint.h>

typedef unsigned short u16;
static constexpr int S = 2048, D = 512, H = 32, DH = 16, F = 1376;

__device__ __forceinline__ float bf2f(u16 b) { return __uint_as_float(((unsigned)b) << 16); }
__device__ __forceinline__ u16 f2bf(float f) { unsigned u = __float_as_uint(f); u += 0x7FFFu + ((u >> 16) & 1u); return (u16)(u >> 16); }
__device__ __forceinline__ float rb(float f) { return bf2f(f2bf(f)); }

__global__ void n_ln(const u16* x, u16* xn, u16* h) {
  int r = blockIdx.x, l = threadIdx.x;
  float v[8];
  for (int i = 0; i < 8; ++i) v[i] = bf2f(x[r * D + l * 8 + i]);
  for (int pass = 0; pass < 2; ++pass) {
    float s = 0; for (int i = 0; i < 8; ++i) s += v[i];
    for (int o = 32; o; o >>= 1) s += __shfl_xor(s, o);
    float m = s / D;
    float q = 0; for (int i = 0; i < 8; ++i) q += (v[i] - m) * (v[i] - m);
    for (int o = 32; o; o >>= 1) q += __shfl_xor(q, o);
    float var = rb(q / D);
    float mb = rb(m);
    float sd = rb(sqrtf(rb(var + 1e-5f)));
    for (int i = 0; i < 8; ++i) v[i] = rb(rb(v[i] - mb) / sd);
    u16* o = pass == 0 ? xn : h;
    for (int i = 0; i < 8; ++i) o[r * D + l * 8 + i] = f2bf(v[i]);
  }
}
__global__ void n_gemm(const u16* A, int sam, int sak, const u16* B, int M, int N, int K, u16* C) {
  int n = blockIdx.x * 64 + threadIdx.x, m = blockIdx.y;
  if (n >= N) return;
  float acc = 0;
  for (int k = 0; k < K; ++k) acc += bf2f(A[(size_t)m * sam + (size_t)k * sak]) * bf2f(B[(size_t)k * N + n]);
  C[(size_t)m * N + n] = f2bf(acc);
}
__global__ void n_phi(const u16* qkv, const u16* wphi, u16* phiq, u16* phik) {
  int idx = blockIdx.x * 64 + threadIdx.x;
  int s = idx >> 6, which = (idx >> 5) & 1, h = idx & 31;
  float q[16], p[16];
  for (int d = 0; d < 16; ++d) q[d] = bf2f(qkv[(size_t)s * 1536 + which * 512 + h * 16 + d]);
  for (int f = 0; f < 16; ++f) { float a = 0; for (int d = 0; d < 16; ++d) a += q[d] * bf2f(wphi[h * 256 + d * 16 + f]); p[f] = rb(a); }
  float mx = -1e30f, mn = 1e30f;
  for (int f = 0; f < 16; ++f) { mx = fmaxf(mx, p[f]); mn = fminf(mn, p[f]); }
  float s1 = 0, s2 = 0, e1[16], e2[16];
  for (int f = 0; f < 16; ++f) { e1[f] = __expf(p[f] - mx); s1 += e1[f]; e2[f] = __expf(mn - p[f]); s2 += e2[f]; }
  u16* o = (which ? phik : phiq) + ((size_t)s * H + h) * 32;
  for (int f = 0; f < 16; ++f) { o[f] = f2bf(e1[f] / s1); o[16 + f] = f2bf(e2[f] / s2); }
}
__global__ void n_kv(const u16* phik, const u16* qkv, float* kv, float* z) {
  int h = blockIdx.x, f = blockIdx.y, d = threadIdx.x;
  if (d > 16) return;
  float a = 0;
  for (int s = 0; s < S; ++s) {
    float p = bf2f(phik[((size_t)s * H + h) * 32 + f]);
    a += d < 16 ? p * bf2f(qkv[(size_t)s * 1536 + 1024 + h * 16 + d]) : p;
  }
  if (d < 16) kv[(h * 32 + f) * 16 + d] = rb(a); else z[h * 32 + f] = rb(a);
}
__global__ void n_o(const u16* phiq, const float* kv, const float* z, u16* o) {
  int idx = blockIdx.x * 64 + threadIdx.x;
  int s = idx >> 9, h = (idx >> 4) & 31, d = idx & 15;
  float num = 0, den = 0;
  for (int f = 0; f < 32; ++f) { float p = bf2f(phiq[((size_t)s * H + h) * 32 + f]); num += p * kv[(h * 32 + f) * 16 + d]; den += p * z[h * 32 + f]; }
  num = rb(num); den = rb(rb(den) + 1e-6f);
  o[idx] = f2bf(num / den);
}
__global__ void n_gelu(u16* u, const u16* b1) {
  int idx = blockIdx.x * 64 + threadIdx.x;
  if (idx >= D * F) return;
  int f = idx % F;
  float v = rb(bf2f(u[idx]) + bf2f(b1[f]));
  float t = tanhf(0.7978845608028654f * (v + 0.044715f * v * v * v));
  u[idx] = f2bf(0.5f * v * (1.f + t));
}
__global__ void n_final(const u16* x, const u16* sA, const u16* y, const u16* b2, u16* out) {
  int idx = blockIdx.x * 64 + threadIdx.x;
  int s = idx >> 9, d = idx & 511;
  float yy = rb(bf2f(y[(size_t)d * S + s]) + bf2f(b2[s]));
  float g = rb(yy / (1.f + __expf(-yy)));
  out[idx] = f2bf(bf2f(x[idx]) + rb(bf2f(sA[idx]) * g));
}

extern "C" void kernel_launch(void* const* d_in, const int* in_sizes, int n_in,
                              void* d_out, int out_size, void* d_ws, size_t ws_size,
                              hipStream_t stream) {
  const u16* x = (const u16*)d_in[0];
  const u16* wqkv = (const u16*)d_in[1];
  const u16* wphi = (const u16*)d_in[2];
  const u16* wo = (const u16*)d_in[3];
  const u16* w1 = (const u16*)d_in[5];
  const u16* b1 = (const u16*)d_in[6];
  const u16* w2 = (const u16*)d_in[7];
  const u16* b2 = (const u16*)d_in[8];
  char* ws = (char*)d_ws;
  u16* xn = (u16*)ws; ws += S * D * 2;
  u16* h = (u16*)ws; ws += S * D * 2;
  u16* qkv = (u16*)ws; ws += S * 1536 * 2;
  u16* phiq = (u16*)ws; ws += S * H * 32 * 2;
  u16* phik = (u16*)ws; ws += S * H * 32 * 2;
  float* kv = (float*)ws; ws += H * 32 * 16 * 4;
  float* z = (float*)ws; ws += H * 32 * 4;
  u16* o = (u16*)ws; ws += S * D * 2;
  u16* sA = (u16*)ws; ws += S * D * 2;
  u16* u = (u16*)ws; ws += D * F * 2;
  u16* y = (u16*)ws; ws += D * S * 2;
  n_ln<<<S, 64, 0, stream>>>(x, xn, h);
  n_gemm<<<dim3(1536 / 64, S), 64, 0, stream>>>(xn, D, 1, wqkv, S, 1536, D, qkv);
  n_phi<<<S, 64, 0, stream>>>(qkv, wphi, phiq, phik);
  n_kv<<<dim3(H, 32), 64, 0, stream>>>(phik, qkv, kv, z);
  n_o<<<S * D / 64, 64, 0, stream>>>(phiq, kv, z, o);
  n_gemm<<<dim3(D / 64, S), 64, 0, stream>>>(o, D, 1, wo, S, D, D, sA);
  n_gemm<<<dim3((F + 63) / 64, D), 64, 0, stream>>>(h, 1, D, w1, D, F, S, u);
  n_gelu<<<(D * F + 63) / 64, 64, 0, stream>>>(u, b1);
  n_gemm<<<dim3(S / 64, D), 64, 0, stream>>>(u, F, 1, w2, D, S, F, y);
  n_final<<<S * D / 64, 64, 0, stream>>>(x, sA, y, b2, (u16*)d_out);
}
```

```cpp
#include <hip/hip_runtime.h>
#include <stdint.h>

namespace hk {
typedef unsigned short u16;
typedef unsigned u32;
typedef unsigned long long u64;
typedef u32 u32x4 __attribute__((ext_vector_type(4)));
typedef u32 u32x2 __attribute__((ext_vector_type(2)));
typedef float f32x4 __attribute__((ext_vector_type(4)));
typedef float f32x2 __attribute__((ext_vector_type(2)));
typedef __bf16 bf16x8 __attribute__((ext_vector_type(8)));
typedef __bf16 bf16x2 __attribute__((ext_vector_type(2)));
typedef short s16x4 __attribute__((ext_vector_type(4)));
#define LAS __attribute__((address_space(3)))
#define GAS __attribute__((address_space(1)))
#define DI __device__ __forceinline__
typedef __amdgpu_buffer_rsrc_t rsrc_t;

#define CROT 0
#define BROT 0
constexpr int S = 2048, D = 512, F = 1376;
constexpr u32 WS_EPOCH = 0;
constexpr u32 FL_SZ = 8 * 256 * 16;
constexpr u32 WS_FH = 4096, WS_FKV = WS_FH + FL_SZ, WS_FO = WS_FKV + FL_SZ, WS_FU = WS_FO + FL_SZ, WS_FCS = WS_FU + FL_SZ;
constexpr u32 WS_STAT = WS_FCS + FL_SZ;
constexpr u32 WS_CS = WS_STAT + S * 8;
constexpr u32 WS_Z = WS_CS + 8192;
constexpr u32 WS_KV = WS_Z + 16 * 32 * 128;
constexpr u32 WS_H = WS_KV + 16 * 32 * 1024;
constexpr u32 WS_O = WS_H + S * D * 2;
constexpr u32 WS_U = WS_O + S * D * 2;
constexpr u32 WS_END = WS_U + D * F * 2;
constexpr u32 WS_STAMP = 64u << 20;
constexpr u32 RING = 147456, L_SCR = RING, L_CTL = 163840 - 256, LDS_BYTES = 163840;
constexpr u32 L_FULL = L_CTL, L_PROG = L_CTL + 16, L_GO = L_CTL + 32, L_GOO = L_CTL + 36, L_LND = L_CTL + 40, L_CNT = L_CTL + 48;
constexpr u32 L_LN = RING - 2048, LN_PITCH = 1088, L_LNS = L_LN + 16 * LN_PITCH + 64;
constexpr u32 L_AKV = 49152, L_AZ = L_AKV + 32768, L_AT = L_AZ + 2048, L_AZT = L_AT + 4096;
constexpr u32 SPIN_G = 1u << 15, SPIN_L = 1u << 20;

DI f32x4 mfma32(u32x4 a, u32x4 b, f32x4 c) {
  return __builtin_amdgcn_mfma_f32_16x16x32_bf16(__builtin_bit_cast(bf16x8, a), __builtin_bit_cast(bf16x8, b), c, 0, 0, 0);
}
DI f32x4 mfma16(u32x2 a, u32x2 b, f32x4 c) {
  return __builtin_amdgcn_mfma_f32_16x16x16bf16_1k(__builtin_bit_cast(s16x4, a), __builtin_bit_cast(s16x4, b), c, 0, 0, 0);
}
DI u32 pk2(float a, float b) { f32x2 v = {a, b}; return __builtin_bit_cast(u32, __builtin_convertvector(v, bf16x2)); }
DI float lo16(u32 p) { return __uint_as_float(p << 16); }
DI float hi16(u32 p) { return __uint_as_float(p & 0xffff0000u); }
DI float bfr(float a) { return lo16(pk2(a, a)); }
DI u32x2 trd(u32 addr) {
  return __builtin_bit_cast(u32x2, __builtin_amdgcn_ds_read_tr16_b64_v4i16((LAS s16x4*)addr));
}
DI u32x4 cat(u32x2 a, u32x2 b) { return u32x4{a.x, a.y, b.x, b.y}; }
DI u32x4 ldsr128(u32 addr) { return *(LAS u32x4*)addr; }
DI void ldsw128(u32 addr, u32x4 v) { *(LAS u32x4*)addr = v; }
DI rsrc_t mkrs(const void* p, u32 bytes) { return __builtin_amdgcn_make_buffer_rsrc((void*)p, 0, bytes, 0x00020000); }
DI u32x4 ld128c(rsrc_t r, u32 vo, u32 so) { return __builtin_amdgcn_raw_buffer_load_b128(r, vo, so, 16); }
DI void st128c(rsrc_t r, u32 vo, u32x4 v) { __builtin_amdgcn_raw_buffer_store_b128(v, r, vo, 0, 16); }
DI void glds(rsrc_t r, u32 vo, u32 so, u32 lds) {
  asm volatile("s_mov_b32 m0, %3\n\ts_nop 0\n\tbuffer_load_dwordx4 %0, %1, %2 offen lds" ::"v"(vo), "s"(r), "s"(so), "s"(lds) : "memory");
}
#define VMCNT(n) asm volatile("s_waitcnt vmcnt(%0)" ::"i"(n) : "memory")
#define BARRIER() asm volatile("s_waitcnt lgkmcnt(0)\n\ts_barrier" ::: "memory")
template <int CTRL> DI float dppf(float v) { return __uint_as_float(__builtin_amdgcn_update_dpp(0u, __float_as_uint(v), CTRL, 0xf, 0xf, true)); }
DI float sum16(float v) { v += dppf<0xB1>(v); v += dppf<0x4E>(v); v += dppf<0x141>(v); v += dppf<0x140>(v); return v; }
DI float xsum(float v) {
  auto a = __builtin_amdgcn_permlane16_swap(__float_as_uint(v), __float_as_uint(v), false, false);
  v = __uint_as_float(a[0]) + __uint_as_float(a[1]);
  auto b = __builtin_amdgcn_permlane32_swap(__float_as_uint(v), __float_as_uint(v), false, false);
  return __uint_as_float(b[0]) + __uint_as_float(b[1]);
}
DI float xmax(float v) {
  auto a = __builtin_amdgcn_permlane16_swap(__float_as_uint(v), __float_as_uint(v), false, false);
  v = fmaxf(__uint_as_float(a[0]), __uint_as_float(a[1]));
  auto b = __builtin_amdgcn_permlane32_swap(__float_as_uint(v), __float_as_uint(v), false, false);
  return fmaxf(__uint_as_float(b[0]), __uint_as_float(b[1]));
}
DI float dot2(u32 a, u32 b, float c) { return __builtin_amdgcn_fdot2_f32_bf16(__builtin_bit_cast(bf16x2, a), __builtin_bit_cast(bf16x2, b), c, false); }
DI float sum32(float v) {
  v = sum16(v);
  auto a = __builtin_amdgcn_permlane16_swap(__float_as_uint(v), __float_as_uint(v), false, false);
  return __uint_as_float(a[0]) + __uint_as_float(a[1]);
}
#define SB() __builtin_amdgcn_sched_barrier(0)
DI u32 opq(u32 v) { asm volatile("" : "+s"(v)); return v; }
DI u32 opv(u32 v) { asm volatile("" : "+v"(v)); return v; }
#define PIN(x) asm volatile("" : "+v"(x))
template <int V> struct IC { static constexpr int v = V; };
template <int I, int N, class Fn> DI void sfor(Fn&& f) { if constexpr (I < N) { f(IC<I>{}); sfor<I + 1, N>(f); } }
DI u32 cw_ld(u32 addr) {
  u32 v; asm volatile("ds_read_b32 %0, %1\n\ts_waitcnt lgkmcnt(0)" : "=v"(v) : "v"(addr) : "memory");
  return __builtin_amdgcn_readfirstlane(v);
}
DI u32 cw_min4(u32 addr) {
  u32x4 v; asm volatile("ds_read_b128 %0, %1\n\ts_waitcnt lgkmcnt(0)" : "=v"(v) : "v"(addr) : "memory");
  return __builtin_amdgcn_readfirstlane(min(min(v.x, v.y), min(v.z, v.w)));
}
DI void cw_st(u32 addr, u32 val, int lane) {
  if (lane == 0) asm volatile("ds_write_b32 %0, %1" ::"v"(addr), "v"(val) : "memory");
}
DI void wait_min4(u32 addr, u32 need) {
#pragma unroll 1
  for (u32 sp = 0; sp < SPIN_L; ++sp) { if (cw_min4(addr) >= need) break; __builtin_amdgcn_s_sleep(1); }
}
DI void wait_word(u32 addr, u32 need) {
#pragma unroll 1
  for (u32 sp = 0; sp < SPIN_L; ++sp) { if (cw_ld(addr) >= need) break; __builtin_amdgcn_s_sleep(1); }
}
DI u32 cw_add(u32 addr, int lane) {
  u32 old = 0;
  if (lane == 0) asm volatile("ds_add_rtn_u32 %0, %1, %2\n\ts_waitcnt lgkmcnt(0)" : "=v"(old) : "v"(addr), "v"(1u) : "memory");
  return __builtin_amdgcn_readfirstlane(old);
}
DI void flag_set(char* ws, u32 base, int id, u64 tg, int lane) {
  if (lane < 8) __hip_atomic_store((GAS u64*)(ws + base + (lane * 256 + id) * 16), tg, __ATOMIC_RELAXED, __HIP_MEMORY_SCOPE_AGENT);
}
template <int N> DI void flag_wait(char* ws, u32 base, int rep, int id0, int id1, u64 tag) {
  GAS u64* p0 = (GAS u64*)(ws + base + (rep * 256 + id0) * 16);
  GAS u64* p1 = (GAS u64*)(ws + base + (rep * 256 + id1) * 16);
#pragma unroll 1
  for (u32 sp = 0; sp < SPIN_G; ++sp) {
    bool ok = __hip_atomic_load(p0, __ATOMIC_RELAXED, __HIP_MEMORY_SCOPE_AGENT) == tag;
    if (N > 1) ok &= __hip_atomic_load(p1, __ATOMIC_RELAXED, __HIP_MEMORY_SCOPE_AGENT) == tag;
    if (__all(ok)) break;
    __builtin_amdgcn_s_sleep(2);
  }
  __builtin_amdgcn_fence(__ATOMIC_ACQUIRE, "wavefront");
  asm volatile("" ::: "memory");
}
DI void publish(char* ws, u32 base, int id, u64 tag, u32 cnt, u32 nw, int lane) {
  VMCNT(0);
  if (cw_add(cnt, lane) == nw - 1) flag_set(ws, base, id, tag, lane);
}

#define ST(i)

struct Ctx {
  const u16 *x, *wqkv, *wphi, *wo, *w1, *b1, *w2, *b2;
  u16* out;
  char* ws;
};

DI u32x4 hedgehog(f32x4 p) {
  const float L2E = 1.4426950408889634f;
  u32 a = pk2(p[0], p[1]), b = pk2(p[2], p[3]);
  float p0 = lo16(a) * L2E, p1 = hi16(a) * L2E, p2 = lo16(b) * L2E, p3 = hi16(b) * L2E;
  float mx = xmax(fmaxf(fmaxf(p0, p1), fmaxf(p2, p3)));
  float mn = -xmax(-fminf(fminf(p0, p1), fminf(p2, p3)));
  float e0 = __builtin_amdgcn_exp2f(p0 - mx), e1 = __builtin_amdgcn_exp2f(p1 - mx), e2 = __builtin_amdgcn_exp2f(p2 - mx), e3 = __builtin_amdgcn_exp2f(p3 - mx);
  float n0 = __builtin_amdgcn_exp2f(mn - p0), n1 = __builtin_amdgcn_exp2f(mn - p1), n2 = __builtin_amdgcn_exp2f(mn - p2), n3 = __builtin_amdgcn_exp2f(mn - p3);
  float rs = __builtin_amdgcn_rcpf(xsum((e0 + e1) + (e2 + e3)));
  float rn = __builtin_amdgcn_rcpf(xsum((n0 + n1) + (n2 + n3)));
  return u32x4{pk2(e0 * rs, e1 * rs), pk2(e2 * rs, e3 * rs), pk2(n0 * rn, n1 * rn), pk2(n2 * rn, n3 * rn)};
}

constexpr int c_need(int n) { return n < 9 ? (n == 0 ? 44 : n <= 2 ? 48 : n == 3 ? 52 : n <= 5 ? 56 : n == 6 ? 60 : 64) : 64 + 4 * (n - 8); }

__global__ __launch_bounds__(512, 2) void fused(const u16* __restrict__ x, const u16* __restrict__ wqkv, const u16* __restrict__ wphi,
                                                const u16* __restrict__ wo, const u16* __restrict__ w1, const u16* __restrict__ b1,
                                                const u16* __restrict__ w2, const u16* __restrict__ b2, u16* __restrict__ out, char* ws) {
#if __HIP_DEVICE_COMPILE__
  ST(0);
  const int tid = threadIdx.x, lane = tid & 63, w = __builtin_amdgcn_readfirstlane(tid >> 6);
  const int l15 = lane & 15, g = lane >> 4;
  const int bid = blockIdx.x, xc = bid & 7, r = bid >> 3;
  const bool isG = w < 4;
  const int hw = w & 3;
  const int stile = 8 * (xc >> 1) + (r & 7), dtile = 4 * (xc & 1) + (r >> 3);
  const bool roleA = r < 16;
  const int j = r & 15;
  const int b_dt = 4 * (xc & 1) + (j & 3), b_ft = 4 * (xc >> 1) + (j >> 2);
  const bool roleB = !roleA && b_ft < 15;
  const int a_rb = 4 * (xc >> 1) + (j & 3), a_grp = 4 * (xc & 1) + (j >> 2);
  const rsrc_t rws = mkrs(ws, WS_END);
  const rsrc_t rx = mkrs(x, S * D * 2);

  const u64 epoch = __hip_atomic_load((GAS u64*)(ws + WS_EPOCH), __ATOMIC_RELAXED, __HIP_MEMORY_SCOPE_AGENT);
#define tag (epoch + 1)
  if (tid < 64) *(LAS u32*)(L_CTL + tid * 4) = 0;
  BARRIER();
  ST(12);

  const u32 cswz = (((lane & 7) ^ (((lane >> 4) & 3) << 1)) << 4);
  const int crot = CROT;
  auto c_issue = [&](int n) {
    const u32 dst = ((6 + n) % 9) * 16384 + hw * 4096;
    if (n < 11) {
      const rsrc_t rw = mkrs(w2, F * S * 2);
      int ks = 4 * n + hw; if (ks > 42) ks = 42;
      ks += crot; if (ks >= 43) ks -= 43;
      const u32 vo = (lane >> 3) * 4096 + cswz + stile * 128;
#pragma unroll
      for (int m = 0; m < 4; ++m) glds(rw, vo, ks * 131072 + m * 32768, dst + m * 1024);
    } else {
      const rsrc_t rw = mkrs(wo, D * D * 2);
      const int kt = 4 * (n - 11) + hw;
      const u32 vo = (lane >> 3) * 1024 + cswz + dtile * 128;
#pragma unroll
      for (int m = 0; m < 4; ++m) glds(rw, vo, kt * 32768 + m * 8192, dst + m * 1024);
    }
  };
  int c_first = 0;
  u32 fullv = 0;
  auto pub = [&](u32 v) { if (v > fullv) { fullv = v; cw_st(L_FULL + hw * 4, v, lane); } };

  if (roleA) {
    const int rq = w & 3, hp = w >> 2;
    const rsrc_t rw = mkrs(wqkv, D * 3 * D * 2);
    const int row0 = a_rb * 128;
    const u32 xvo = (row0 + 16 * w + (lane >> 3)) * 1024, xsw = lane >> 4;
    const u32 wvo = (lane >> 3) * 3072 + (((lane & 7) ^ ((((lane >> 4) & 1) | ((w & 1) << 1)) << 1)) << 4) + a_grp * 128;
    const u32 xv0 = xvo + (((lane & 7) ^ xsw) << 4), xv1 = xvo + (((lane & 7) ^ (xsw + 4)) << 4);
    auto a_issue1 = [&](int c, int i) {
      const u32 dst = (c % 3) * 40960;
      if (i < 2) glds(rx, i ? xv1 : xv0, i * 8192 + c * 128, dst + w * 2048 + i * 1024);
      else glds(rw, wvo, (64 * c + 8 * w) * 3072 + (i - 2) * 1024, dst + 16384 + (i - 2) * 8192 + w * 1024);
    };
    u32 wpr[2][4];
#pragma unroll
    for (int e = 0; e < 2; ++e) {
      const u16* p = wphi + (a_grp * 4 + 2 * hp + e) * 256 + 4 * g * 16 + l15;
#pragma unroll
      for (int i = 0; i < 4; ++i) wpr[e][i] = p[16 * i];
    }
    SB();
#pragma unroll
    for (int i = 0; i < 10; ++i) a_issue1(i / 5, i % 5);
    ST(13);
    f32x4 aq[2][2], ak[2][2], av[2][2];
#pragma unroll
    for (int a = 0; a < 2; ++a)
#pragma unroll
      for (int e = 0; e < 2; ++e) { aq[a][e] = f32x4{0, 0, 0, 0}; ak[a][e] = aq[a][e]; av[a][e] = aq[a][e]; }
    const u32 xb0 = (32 * rq + l15) * 128 + ((g ^ ((l15 >> 1) & 7)) << 4);
    const u32 wf = ((l15 >> 3) & 1) | ((g & 1) << 1);
    u32 wb[2];
#pragma unroll
    for (int e = 0; e < 2; ++e) wb[e] = 16384 + (8 * g + (l15 >> 2)) * 128 + (((2 * hp + e) ^ wf) << 5) + (l15 & 3) * 8;
    u32x4 xa[2][2], wa[2][6];
    auto a_step = [&](bool mma, int kc, bool rd, int kr, u32 sb, int cn, int p0, int np) {
      u32 xb = 0, wbb[2] = {0, 0};
      if (rd) { xb = opv(sb + (xb0 ^ (kr * 64))); wbb[0] = opv(sb + wb[0]); wbb[1] = opv(sb + wb[1]); }
#pragma unroll
      for (int q = 0; q < 6; ++q) {
        const int e = q / 3, sec = q % 3;
        if (rd) {
          if (q < 2) xa[kr][q] = ldsr128(xb + q * 2048);
          const u32 ad = wbb[e] + sec * 8192 + kr * 4096;
          wa[kr][q] = cat(trd(ad), trd(ad + 512));
        }
        SB();
        if (mma) {
#pragma unroll
          for (int a = 0; a < 2; ++a) {
            if (sec == 0) { aq[a][e] = mfma32(wa[kc][q], xa[kc][a], aq[a][e]); PIN(aq[a][e]); }
            if (sec == 1) { ak[a][e] = mfma32(wa[kc][q], xa[kc][a], ak[a][e]); PIN(ak[a][e]); }
            if (sec == 2) { av[a][e] = mfma32(xa[kc][a], wa[kc][q], av[a][e]); PIN(av[a][e]); }
          }
        }
        if ((q & 1) && q / 2 < np) a_issue1(cn, p0 + q / 2);
        SB();
      }
    };
    u64 early = 0;
    GAS u64* const pfl = (GAS u64*)(ws + (lane < 8 ? WS_FH + (xc * 256 + a_rb * 8 + lane) * 16 : WS_FCS + (xc * 256 + a_grp) * 16));
    u32x2 stl[2]; u32x4 stv[2][2], csq[2], csk[2]; u32 csv[2];
#pragma unroll
    for (int c = 0; c < 8; ++c) {
      if (c == 7 && w == 0) {
        VMCNT(0);
        asm volatile("" : "+v"(early));
        if (!__all(early == tag)) flag_wait<1>(ws, lane < 8 ? WS_FH : WS_FCS, xc, lane < 8 ? a_rb * 8 + lane : a_grp, 0, tag);
      }
      if (c < 7) VMCNT(5); else VMCNT(0);
      BARRIER();
      ST(16 + c);
      const u32 sb = (c % 3) * 40960;
      if (c == 7) {
#pragma unroll
        for (int a = 0; a < 2; ++a) {
          const u32 rb8 = WS_STAT + (row0 + 32 * rq + 16 * a) * 8;
          stl[a] = __builtin_amdgcn_raw_buffer_load_b64(rws, rb8 + l15 * 8, 0, 16);
          stv[a][0] = ld128c(rws, rb8 + g * 32, 0);
          stv[a][1] = ld128c(rws, rb8 + g * 32 + 16, 0);
        }
#pragma unroll
        for (int e = 0; e < 2; ++e) {
          const u32 cb = WS_CS + (a_grp * 64 + (2 * hp + e) * 16) * 4;
          csq[e] = ld128c(rws, cb + g * 16, 0);
          csk[e] = ld128c(rws, cb + 2048 + g * 16, 0);
          csv[e] = __builtin_amdgcn_raw_buffer_load_b32(rws, cb + 4096 + l15 * 4, 0, 16);
        }
      }
      if (c == 6 && w == 0) asm volatile("global_load_dwordx2 %0, %1, off sc1" : "=v"(early) : "v"(pfl) : "memory");
      a_step(c > 0, 1, true, 0, sb, c + 2, 0, c < 6 ? 2 : 0);
      a_step(true, 0, true, 1, sb, c + 2, 2, c < 6 ? 3 : 0);
    }
    a_step(true, 1, false, 0, 0, 0, 0, 0);
    u32x2 wph[2];
#pragma unroll
    for (int e = 0; e < 2; ++e) wph[e] = u32x2{wpr[e][0] | (wpr[e][1] << 16), wpr[e][2] | (wpr[e][3] << 16)};
    ST(2);
    BARRIER();
    const u32 one2 = 0x3f803f80u;
    u32x4 selP = {0, 0, 0, 0}, selN = {0, 0, 0, 0};
    {
      const int dlt = l15 - 4 * g;
      const u32 v0 = dlt == 0 ? 0x3f80u : dlt == 1 ? 0x3f800000u : 0u, v1 = dlt == 2 ? 0x3f80u : dlt == 3 ? 0x3f800000u : 0u;
      selP.x = v0; selP.y = v1; selN.z = v0; selN.w = v1;
    }
    auto fold = [&](f32x4 acc, float m, float ri, u32x4 cs) {
      return u32x2{pk2((acc[0] - m * __uint_as_float(cs.x)) * ri, (acc[1] - m * __uint_as_float(cs.y)) * ri),
                   pk2((acc[2] - m * __uint_as_float(cs.z)) * ri, (acc[3] - m * __uint_as_float(cs.w)) * ri)};
    };
    const f32x4 zero4 = {0, 0, 0, 0};
#pragma unroll
    for (int e = 0; e < 2; ++e) {
      u32x2 tp[2], tn[2], vb[2];
#pragma unroll
      for (int a = 0; a < 2; ++a) {
        const u32x4 ph = hedgehog(mfma16(wph[e], fold(ak[a][e], __uint_as_float(stl[a].x), __uint_as_float(stl[a].y), csk[e]), zero4));
        const f32x4 p4 = mfma32(ph, selP, zero4), n4 = mfma32(ph, selN, zero4);
        tp[a] = u32x2{pk2(p4[0], p4[1]), pk2(p4[2], p4[3])};
        tn[a] = u32x2{pk2(n4[0], n4[1]), pk2(n4[2], n4[3])};
        const float cv = __uint_as_float(csv[e]);
        const f32x4 t = av[a][e];
        vb[a] = u32x2{pk2((t[0] - __uint_as_float(stv[a][0].x) * cv) * __uint_as_float(stv[a][0].y), (t[1] - __uint_as_float(stv[a][0].z) * cv) * __uint_as_float(stv[a][0].w)),
                      pk2((t[2] - __uint_as_float(stv[a][1].x) * cv) * __uint_as_float(stv[a][1].y), (t[3] - __uint_as_float(stv[a][1].z) * cv) * __uint_as_float(stv[a][1].w))};
      }
      const u32x4 P = cat(tp[0], tp[1]), N = cat(tn[0], tn[1]), V = cat(vb[0], vb[1]), ones = {one2, one2, one2, one2};
      const f32x4 kvp = mfma32(P, V, zero4), kvn = mfma32(N, V, zero4), zp = mfma32(P, ones, zero4), zn = mfma32(N, ones, zero4);
      const int ti = (2 * hp + e) * 2;
      ldsw128(L_AKV + (ti * 4 + rq) * 1024 + lane * 16, __builtin_bit_cast(u32x4, kvp));
      ldsw128(L_AKV + ((ti + 1) * 4 + rq) * 1024 + lane * 16, __builtin_bit_cast(u32x4, kvn));
      if (l15 == 0) {
        ldsw128(L_AZ + (ti * 4 + rq) * 64 + g * 16, __builtin_bit_cast(u32x4, zp));
        ldsw128(L_AZ + ((ti + 1) * 4 + rq) * 64 + g * 16, __builtin_bit_cast(u32x4, zn));
      }
    }
    BARRIER();
    ST(3);
    if (isG) {
      f32x4 sp = zero4, sn = zero4;
      float zz = 0.f;
#pragma unroll
      for (int q = 0; q < 4; ++q) {
        sp += __builtin_bit_cast(f32x4, ldsr128(L_AKV + (w * 8 + q) * 1024 + lane * 16));
        sn += __builtin_bit_cast(f32x4, ldsr128(L_AKV + (w * 8 + 4 + q) * 1024 + lane * 16));
        zz += *(LAS float*)(L_AZ + (w * 8 + ((lane >> 4) & 1) * 4 + q) * 64 + l15 * 4);
      }
      const u32 rec = a_rb * 32 + a_grp * 4 + w;
      st128c(rws, WS_KV + rec * 1024 + lane * 16, u32x4{pk2(sp[0], sp[1]), pk2(sp[2], sp[3]), pk2(sn[0], sn[1]), pk2(sn[2], sn[3])});
      if (lane < 32) __builtin_amdgcn_raw_buffer_store_b32(__float_as_uint(zz), rws, WS_Z + rec * 128 + lane * 4, 0, 16);
      publish(ws, WS_FKV, a_rb * 8 + a_grp, tag, L_CNT, 4, lane);
    } else {
#pragma unroll
      for (int n = 0; n < 6; ++n) c_issue(n);
      c_first = 6;
    }
    ST(4);
    u32x4 phq[2][2];
#pragma unroll
    for (int a = 0; a < 2; ++a)
#pragma unroll
      for (int e = 0; e < 2; ++e)
        phq[a][e] = hedgehog(mfma16(wph[e], fold(aq[a][e], __uint_as_float(stl[a].x), __uint_as_float(stl[a].y), csq[e]), zero4));
    ST(5);
    if (isG) {
      if (w == 0) { flag_wait<1>(ws, WS_FKV, xc, (lane & 15) * 8 + a_grp, 0, tag); cw_st(L_LND, 1, lane); }
      else wait_word(L_LND, 1);
      ST(6);
      u32x4 kvl[16]; u32 zl[16];
#pragma unroll
      for (int q = 0; q < 16; ++q) {
        const u32 rec = q * 32 + a_grp * 4 + w;
        kvl[q] = ld128c(rws, WS_KV + lane * 16, rec * 1024);
        zl[q] = __builtin_amdgcn_raw_buffer_load_b32(rws, WS_Z + (lane & 31) * 4, rec * 128, 16);
      }
      SB();
      const u32 selo = opv(0x3f80u), sehi = opv(0x3f800000u);
      float a8[8], zz = 0.f;
#pragma unroll
      for (int i = 0; i < 8; ++i) a8[i] = 0.f;
#pragma unroll
      for (int q = 0; q < 16; ++q) {
#pragma unroll
        for (int i = 0; i < 4; ++i) { a8[2 * i] = dot2(kvl[q][i], selo, a8[2 * i]); a8[2 * i + 1] = dot2(kvl[q][i], sehi, a8[2 * i + 1]); }
        zz += __uint_as_float(zl[q]);
      }
      ldsw128(L_AT + w * 1024 + lane * 16, u32x4{pk2(a8[0], a8[1]), pk2(a8[2], a8[3]), pk2(a8[4], a8[5]), pk2(a8[6], a8[7])});
      if (lane < 32) *(LAS u16*)(L_AZT + w * 64 + lane * 2) = (u16)pk2(zz, zz);
    }
    BARRIER();
    ST(14);
    {
      u32x4 akv[2], az[2];
#pragma unroll
      for (int e = 0; e < 2; ++e) {
        akv[e] = ldsr128(L_AT + (2 * hp + e) * 1024 + lane * 16);
        az[e] = cat(*(LAS u32x2*)(L_AZT + (2 * hp + e) * 64 + g * 8), *(LAS u32x2*)(L_AZT + (2 * hp + e) * 64 + 32 + g * 8));
      }
#pragma unroll
      for (int a = 0; a < 2; ++a) {
        u32x2 ov[2];
#pragma unroll
        for (int e = 0; e < 2; ++e) {
          const f32x4 num = mfma32(akv[e], phq[a][e], zero4), den = mfma32(az[e], phq[a][e], zero4);
          const float rd = __builtin_amdgcn_rcpf(bfr(bfr(den[0]) + 1e-6f));
          const u32 n01 = pk2(num[0], num[1]), n23 = pk2(num[2], num[3]);
          ov[e] = u32x2{pk2(lo16(n01) * rd, hi16(n01) * rd), pk2(lo16(n23) * rd, hi16(n23) * rd)};
        }
        st128c(rws, WS_O + ((a_grp * 2 + hp) * 128 + a_rb * 8 + 2 * rq + a) * 1024 + lane * 16, cat(ov[0], ov[1]));
      }
      publish(ws, WS_FO, a_rb * 8 + a_grp, tag, L_CNT + 4, 8, lane);
      if (!isG) pub(16 + 6);
    }
    BARRIER();
    if (isG) cw_st(L_PROG + hw * 4, 64, lane);
    ST(7);
  } else {
    const int li = xc * 16 + j;
    const int rot = BROT;
    const rsrc_t rw1 = mkrs(w1, S * F * 2);
    u32 vo[6];
    u32x4 cst[24];
    auto b_issue = [&](int m) {
      const u32 so = ((4 * m + hw + rot) & 63) * (32 * F * 2);
#pragma unroll
      for (int n = 0; n < 6; ++n) glds(rw1, vo[n], so, (m % 6) * 24576 + hw * 6144 + n * 1024);
    };
    {
      const int rl = 2 * w + (lane >> 5), row = li * 16 + rl;
      u32x4 xr[2];
      asm volatile("buffer_load_dwordx4 %0, %2, %3, 0 offen\n\tbuffer_load_dwordx4 %1, %2, %3, 0 offen offset:512"
                   : "=&v"(xr[0]), "=&v"(xr[1]) : "v"(row * 1024 + (lane & 31) * 16), "s"(rx) : "memory");
      if (!isG && roleB) {
#pragma unroll
        for (int n = 0; n < 6; ++n) {
          const int c = n * 64 + lane, rr = c / 12;
          int ch = (c % 12) ^ (((rr >> 3) & 1) << 1);
          if (b_ft == 14) ch &= 3;
          vo[n] = rr * (F * 2) + ch * 16 + b_ft * 192;
        }
        b_issue(0);
        ST(13);
        VMCNT(6);
      } else if (!roleB) {
        const rsrc_t rw = mkrs(wqkv, D * 3 * D * 2);
#pragma unroll
        for (int i = 0; i < 24; ++i)
          cst[i] = __builtin_amdgcn_raw_buffer_load_b128(rw, (w * 64 + (lane >> 3)) * 3072 + ((xc & 1) * 4 + (j & 3)) * 128 + (lane & 7) * 16, (i / 3) * 8 * 3072 + (i % 3) * 1024, 0);
        SB();
        VMCNT(24);
      } else {
        ST(13);
        VMCNT(0);
      }
      asm volatile("" : "+v"(xr[0]), "+v"(xr[1]));
      ST(1);
      u32 pk[8] = {xr[0].x, xr[0].y, xr[0].z, xr[0].w, xr[1].x, xr[1].y, xr[1].z, xr[1].w};
#pragma unroll
      for (int pass = 0; pass < 2; ++pass) {
        float s1 = 0.f, s2 = 0.f;
#pragma unroll
        for (int i = 0; i < 8; ++i) { s1 = dot2(pk[i], 0x3f803f80u, s1); s2 = dot2(pk[i], pk[i], s2); }
        const float m = sum32(s1) * (1.f / D);
        const float var = bfr(fmaxf(sum32(s2) * (1.f / D) - m * m, 0.f));
        const float mb = bfr(m);
        const float ri = __builtin_amdgcn_rcpf(bfr(__builtin_amdgcn_sqrtf(bfr(var + 1e-5f))));
#pragma unroll
        for (int i = 0; i < 8; ++i) pk[i] = pk2((lo16(pk[i]) - mb) * ri, (hi16(pk[i]) - mb) * ri);
        if (pass == 0 && (lane & 31) == 0) *(LAS u32x2*)(L_LNS + rl * 8) = u32x2{__float_as_uint(mb), __float_as_uint(ri)};
      }
      const u32 wa = L_LN + rl * LN_PITCH + (rl >> 3) * 32 + (lane & 31) * 16;
      ldsw128(wa, u32x4{pk[0], pk[1], pk[2], pk[3]});
      ldsw128(wa + 512, u32x4{pk[4], pk[5], pk[6], pk[7]});
      cw_add(L_CNT + 8, lane);
      ST(14);
    }
    if (isG) {
      wait_word(L_CNT + 8, 8);
      ST(15);
#pragma unroll
      for (int rr = 0; rr < 4; ++rr) {
        const int item = w * 16 + rr * 4 + g, dt = item >> 1, oct = item & 1;
        const u32 ad = L_LN + (8 * oct + (l15 >> 2)) * LN_PITCH + oct * 32 + dt * 32 + (l15 & 3) * 8;
        const u32x4 hv = cat(trd(ad), trd(ad + 4 * LN_PITCH));
        st128c(rws, WS_H + (((li >> 1) * 32 + dt) * 4 + (li & 1) * 2 + oct) * 256 + l15 * 16, hv);
      }
      if (w == 0 && lane < 8) st128c(rws, WS_STAT + li * 128 + lane * 16, ldsr128(L_LNS + lane * 16));
      ST(2);
      VMCNT(0);
      if (cw_add(L_CNT + 12, lane) == 3) { flag_set(ws, WS_FH, li, tag, lane); cw_st(L_LND, 1, lane); }
      ST(3);
    }
    if (!roleB) {
      const int grp = (xc & 1) * 4 + (j & 3);
      const u32 sv = (l15 & 1) ? 0x3f800000u : 0x3f80u;
      const u32x4 sel = {l15 >> 1 == 0 ? sv : 0u, l15 >> 1 == 1 ? sv : 0u, l15 >> 1 == 2 ? sv : 0u, l15 >> 1 == 3 ? sv : 0u};
      f32x4 cs[3] = {{0, 0, 0, 0}, {0, 0, 0, 0}, {0, 0, 0, 0}};
#pragma unroll
      for (int i = 0; i < 24; ++i) cs[i % 3] = mfma32(cst[i], sel, cs[i % 3]);
#pragma unroll
      for (int sec = 0; sec < 3; ++sec) ldsw128((w * 3 + sec) * 1024 + lane * 16, __builtin_bit_cast(u32x4, cs[sec]));
      if (cw_add(L_CNT, lane) == 7) {
        const int pos = lane >> 3, jj = lane & 7;
        float o3[3];
#pragma unroll
        for (int sec = 0; sec < 3; ++sec) {
          float a = 0.f;
#pragma unroll
          for (int q = 0; q < 8; ++q) {
            const u32 ad = (q * 3 + sec) * 1024 + ((pos >> 2) * 16 + jj) * 16 + (pos & 3) * 4;
            a += *(LAS float*)ad + *(LAS float*)(ad + 512);
          }
          o3[sec] = a;
        }
#pragma unroll
        for (int sec = 0; sec < 3; ++sec) __builtin_amdgcn_raw_buffer_store_b32(__float_as_uint(o3[sec]), rws, WS_CS + sec * 2048 + grp * 256 + lane * 4, 0, 16);
        VMCNT(0);
        flag_set(ws, WS_FCS, grp, tag, lane);
        cw_st(L_CNT + 4, 1, lane);
        ST(4);
      }
    }
    if (isG) {
    if (roleB) {
      u32x2 bias[6];
#pragma unroll
      for (int t = 0; t < 6; ++t) { int f = b_ft * 96 + t * 16 + 4 * g; if (f > F - 4) f = F - 4; bias[t] = *(const GAS u32x2*)(b1 + f); }
      if (w == 0) {
        flag_wait<2>(ws, WS_FH, xc, lane, lane + 64, tag);
        cw_st(L_GO, 1, lane);
      } else {
        wait_word(L_GO, 1);
      }
      ST(4);
      const u32 hvo = (b_dt * 4 + w) * 1024 + lane * 16;
      constexpr int P = 16;
      u32x4 hr[P];
#pragma unroll
      for (int i = 0; i < P; ++i) hr[i] = ld128c(rws, hvo, WS_H + ((i + rot) & 63) * 32768);
      __builtin_amdgcn_sched_barrier(0);
      f32x4 acc[6];
#pragma unroll
      for (int t = 0; t < 6; ++t) acc[t] = f32x4{0, 0, 0, 0};
      const u32 rowb = (8 * g + (l15 >> 2)) * 192 + (l15 & 3) * 8;
      const u32 bE = rowb + (g & 1) * 32, bO = rowb - (g & 1) * 32;
      u32x4 wfr[2][6];
      u32 avail = 0;
      auto b_need = [&](u32 need) { if (avail < need) { wait_min4(L_FULL, need); avail = cw_min4(L_FULL); } };
      u32 gE = bE, gO = bO;
      auto b_rd = [&](int buf, int k, int t) {
        const u32 ad = ((t & 1) ? gO : gE) + k * 6144 + t * 32;
        wfr[buf][t] = cat(trd(ad), trd(ad + 768));
      };
      b_need(1);
#pragma unroll
      for (int t = 0; t < 6; ++t) b_rd(0, 0, t);
      u32 pos = 0;
#pragma unroll 1
      for (int mo = 0; mo < 4; ++mo) {
        const u32 hv2 = mo < 3 ? hvo : 0x80000000u;
#pragma unroll
        for (int mi = 0; mi < 4; ++mi) {
          const int m = mo * 4 + mi;
          ST(16 + mi);
#pragma unroll
          for (int k = 0; k < 4; ++k) {
            if (k == 3) {
              cw_st(L_PROG + w * 4, 4 * (m + 1), lane);
              pos = pos == 5 ? 0 : pos + 1; gE = opv(bE + pos * 24576); gO = opv(bO + pos * 24576);
              b_need(m < 15 ? m + 2 : 0);
            }
#pragma unroll
            for (int t = 0; t < 6; ++t) {
              b_rd((k + 1) & 1, (k + 1) & 3, t);
              SB();
              acc[t] = mfma32(wfr[k & 1][t], hr[mi * 4 + k], acc[t]);
              PIN(acc[t]);
              SB();
            }
            hr[mi * 4 + k] = ld128c(rws, hv2, WS_H + ((m * 4 + k + P + rot) & 63) * 32768);
            SB();
          }
        }
      }
      ST(5);
#pragma unroll
      for (int m3 = 0; m3 < 3; ++m3) {
        u32x4 uv;
#pragma unroll
        for (int h2 = 0; h2 < 2; ++h2) {
          const int t = 2 * m3 + h2;
          float o4[4];
#pragma unroll
          for (int i = 0; i < 4; ++i) {
            const u32 bw = i < 2 ? bias[t].x : bias[t].y;
            const float v = acc[t][i] + ((i & 1) ? hi16(bw) : lo16(bw));
            const float zz = v * (1.f + 0.044715f * v * v) * (-2.f * 0.7978845608028654f * 1.4426950408889634f);
            o4[i] = v * __builtin_amdgcn_rcpf(1.f + __builtin_amdgcn_exp2f(zz));
          }
          uv[2 * h2] = pk2(o4[0], o4[1]); uv[2 * h2 + 1] = pk2(o4[2], o4[3]);
        }
        if (b_ft * 3 + m3 < 43) st128c(rws, WS_U + ((b_ft * 3 + m3) * 32 + b_dt * 4 + w) * 1024 + lane * 16, uv);
      }
      ST(6);
      publish(ws, WS_FU, b_dt * 16 + b_ft, tag, L_CNT, 4, lane);
      ST(7);
    } else {
      wait_word(L_LND, 1);
      cw_st(L_PROG + w * 4, 64, lane);
    }
    } else if (roleB) {
#pragma unroll
    for (int m = 1; m < 16; ++m) {
      if (m == 5) wait_word(L_LND, 1);
      if (m >= 6) {
        if (cw_min4(L_PROG) < 4 * (m - 5)) { VMCNT(0); pub(m); wait_min4(L_PROG, 4 * (m - 5)); }
      }
      b_issue(m);
      if (m >= 3) { VMCNT(18); pub(m - 2); }
      ST(16 + m);
    }
    VMCNT(12); pub(14); VMCNT(6); pub(15); VMCNT(0); pub(16);
    ST(4);
  } else {
    wait_word(L_CNT + 4, 1);
    }
  }

  if (isG) {
    const int srow = stile * 64 + l15;
    u32x2 xin[4]; u32 b2v[4];
#pragma unroll
    for (int t = 0; t < 4; ++t) {
      xin[t] = *(const GAS u32x2*)(x + (srow + 16 * t) * D + dtile * 64 + w * 16 + 4 * g);
      b2v[t] = b2[srow + 16 * t];
    }
    if (w == 0) {
      flag_wait<1>(ws, WS_FU, xc, dtile * 16 + (l15 < 15 ? l15 : 0), 0, tag);
      cw_st(L_GO, 2, lane);
    } else {
      wait_word(L_GO, 2);
    }
    ST(8);
    constexpr int P = 16;
    const u32 uvo = (dtile * 4 + w) * 1024 + lane * 16, ovo = (stile * 4 + w) * 1024 + lane * 16;
    auto uoff = [&](int i) { int ks = i + crot; if (ks >= 43) ks -= 43; return WS_U + ks * 32768; };
    u32x4 ar[P];
#pragma unroll
    for (int i = 0; i < P; ++i) ar[i] = ld128c(rws, uvo, uoff(i));
    __builtin_amdgcn_sched_barrier(0);
    f32x4 ay[4], as[4];
#pragma unroll
    for (int t = 0; t < 4; ++t) { ay[t] = f32x4{0, 0, 0, 0}; as[t] = ay[t]; }
    u32 tb[4];
    {
      const int row = 4 * g + (l15 >> 2), f = (row >> 1) & 3;
#pragma unroll
      for (int t = 0; t < 4; ++t) tb[t] = row * 128 + ((t ^ f) << 5) + (l15 & 3) * 8;
    }
    u32x4 wfr[3][4];
    u32 avail = 0;
    auto c_needf = [&](u32 need) { if (avail < need) { wait_min4(L_FULL, need); avail = cw_min4(L_FULL); } };
    u32 tg[4];
    auto c_pre = [&](auto ic) {
      constexpr int i = decltype(ic)::v;
      if (i < 60 && (i & 3) == 0) {
        c_needf(16 + i / 4 + 1);
#pragma unroll
        for (int t = 0; t < 4; ++t) tg[t] = opv(tb[t] + ((6 + i / 4) % 9) * 16384);
      }
    };
    auto c_rd = [&](auto ic, int t) {
      constexpr int i = decltype(ic)::v;
      if (i == 43 || i >= 60) return;
      const u32 ad = tg[t] + (i & 3) * 4096;
      wfr[i % 3][t] = cat(trd(ad), trd(ad + 2048));
    };
    c_pre(IC<0>{});
#pragma unroll
    for (int t = 0; t < 4; ++t) { c_rd(IC<0>{}, t); }
#pragma unroll
    for (int t = 0; t < 4; ++t) { c_rd(IC<1>{}, t); }
    sfor<0, 60>([&](auto ic) {
      constexpr int i = decltype(ic)::v;
      if ((i & 3) == 0) ST(32 + i / 4);
      if (i == 36) {
        wait_word(L_GOO, 1);
        ST(9);
#pragma unroll
        for (int q = 44; q < 52; ++q) ar[q % P] = ld128c(rws, ovo, WS_O + (q - 44) * 131072);
      }
      c_pre(IC<i + 2>{});
#pragma unroll
      for (int t = 0; t < 4; ++t) {
        c_rd(IC<i + 2>{}, t);
        SB();
        if (i < 43) { ay[t] = mfma32(ar[i % P], wfr[i % 3][t], ay[t]); PIN(ay[t]); }
        if (i > 43) { as[t] = mfma32(wfr[i % 3][t], ar[i % P], as[t]); PIN(as[t]); }
        SB();
      }
      if (((i + 2) & 3) == 3 && i + 2 < 60) cw_st(L_PROG + w * 4, 64 + 4 * ((i + 2) / 4 + 1), lane);
      constexpr int nx = i + P;
      if (nx < 43) ar[i % P] = ld128c(rws, uvo, uoff(nx));
      else if (nx >= 52 && nx < 60) ar[i % P] = ld128c(rws, ovo, WS_O + (nx - 44) * 131072);
      SB();
    });
    ST(10);
#pragma unroll
    for (int t = 0; t < 4; ++t)
      if (t != w) ldsw128(L_SCR + (t * 4 + w) * 1024 - (w > t ? 1024 : 0) - t * 1024 + lane * 16, __builtin_bit_cast(u32x4, as[t]));
    BARRIER();
#pragma unroll
    for (int t = 0; t < 4; ++t) {
      f32x4 sv;
      if (t == w) sv = w == 0 ? as[0] : w == 1 ? as[1] : w == 2 ? as[2] : as[3];
      else sv = __builtin_bit_cast(f32x4, ldsr128(L_SCR + (w * 3 + t - (t > w ? 1 : 0)) * 1024 + lane * 16));
      const float bb = lo16(b2v[t]);
      float o4[4];
#pragma unroll
      for (int i = 0; i < 4; ++i) {
        const float yy = ay[t][i] + bb;
        const float gg = yy * __builtin_amdgcn_rcpf(1.f + __builtin_amdgcn_exp2f(yy * -1.4426950408889634f));
        const u32 xw = i < 2 ? xin[t].x : xin[t].y;
        o4[i] = ((i & 1) ? hi16(xw) : lo16(xw)) + sv[i] * gg;
      }
      *(GAS u32x2*)(out + (srow + 16 * t) * D + dtile * 64 + w * 16 + 4 * g) = u32x2{pk2(o4[0], o4[1]), pk2(o4[2], o4[3])};
    }
  } else {
#pragma unroll
    for (int n = 0; n < 15; ++n) {
      if (n >= c_first) {
        if (cw_min4(L_PROG) < c_need(n)) { VMCNT(0); pub(16 + n); wait_min4(L_PROG, c_need(n)); }
        c_issue(n);
        VMCNT(12);
        pub(16 + n - 2);
        ST(32 + n);
      }
    }
    VMCNT(0);
    pub(31);
    ST(8);
    if (w == 4) {
      flag_wait<1>(ws, WS_FO, xc, (stile >> 1) * 8 + (lane & 7), 0, tag);
      cw_st(L_GOO, 1, lane);
      if (bid == 0 && lane == 0) __hip_atomic_store((GAS u64*)(ws + WS_EPOCH), tag, __ATOMIC_RELAXED, __HIP_MEMORY_SCOPE_AGENT);
    }
    ST(9);
    BARRIER();
  }
#endif
}
#undef tag
}

extern "C" void kernel_launch(void* const* d_in, const int* in_sizes, int n_in, void* d_out, int out_size, void* d_ws, size_t ws_size,
                              hipStream_t stream) {
  using namespace hk;
  (void)hipFuncSetAttribute((const void*)fused, hipFuncAttributeMaxDynamicSharedMemorySize, LDS_BYTES);
  fused<<<256, 512, LDS_BYTES, stream>>>((const u16*)d_in[0], (const u16*)d_in[1], (const u16*)d_in[2], (const u16*)d_in[3], (const u16*)d_in[5],
                                         (const u16*)d_in[6], (const u16*)d_in[7], (const u16*)d_in[8], (u16*)d_out, (char*)d_ws);
}
```

```cpp
#define STAMPS
#define AMP 0
#define AMP_BID 128
#define AMP_W 0
#include <hip/hip_runtime.h>
#include <stdint.h>

namespace hk {
typedef unsigned short u16;
typedef unsigned u32;
typedef unsigned long long u64;
typedef u32 u32x4 __attribute__((ext_vector_type(4)));
typedef u32 u32x2 __attribute__((ext_vector_type(2)));
typedef float f32x4 __attribute__((ext_vector_type(4)));
typedef float f32x2 __attribute__((ext_vector_type(2)));
typedef __bf16 bf16x8 __attribute__((ext_vector_type(8)));
typedef __bf16 bf16x2 __attribute__((ext_vector_type(2)));
typedef short s16x4 __attribute__((ext_vector_type(4)));
#define LAS __attribute__((address_space(3)))
#define GAS __attribute__((address_space(1)))
#define DI __device__ __forceinline__
typedef __amdgpu_buffer_rsrc_t rsrc_t;

#define CROT 0
#define BROT 0
constexpr int S = 2048, D = 512, F = 1376;
constexpr u32 WS_EPOCH = 0;
constexpr u32 FL_SZ = 8 * 256 * 16;
constexpr u32 WS_FH = 4096, WS_FKV = WS_FH + FL_SZ, WS_FO = WS_FKV + FL_SZ, WS_FU = WS_FO + FL_SZ, WS_FCS = WS_FU + FL_SZ;
constexpr u32 WS_STAT = WS_FCS + FL_SZ;
constexpr u32 WS_CS = WS_STAT + S * 8;
constexpr u32 WS_Z = WS_CS + 8192;
constexpr u32 WS_KV = WS_Z + 16 * 32 * 128;
constexpr u32 WS_H = WS_KV + 16 * 32 * 1024;
constexpr u32 WS_O = WS_H + S * D * 2;
constexpr u32 WS_U = WS_O + S * D * 2;
constexpr u32 WS_END = WS_U + D * F * 2;
constexpr u32 WS_STAMP = 64u << 20;
constexpr u32 RING = 147456, L_SCR = RING, L_CTL = 163840 - 256, LDS_BYTES = 163840;
constexpr u32 L_FULL = L_CTL, L_PROG = L_CTL + 16, L_GO = L_CTL + 32, L_GOO = L_CTL + 36, L_LND = L_CTL + 40, L_CNT = L_CTL + 48;
constexpr u32 L_LN = RING - 2048, LN_PITCH = 1088, L_LNS = L_LN + 16 * LN_PITCH + 64;
constexpr u32 L_AKV = 49152, L_AZ = L_AKV + 32768, L_AT = L_AZ + 2048, L_AZT = L_AT + 4096;
constexpr u32 SPIN_G = 1u << 15, SPIN_L = 1u << 20;

DI f32x4 mfma32(u32x4 a, u32x4 b, f32x4 c) {
  return __builtin_amdgcn_mfma_f32_16x16x32_bf16(__builtin_bit_cast(bf16x8, a), __builtin_bit_cast(bf16x8, b), c, 0, 0, 0);
}
DI f32x4 mfma16(u32x2 a, u32x2 b, f32x4 c) {
  return __builtin_amdgcn_mfma_f32_16x16x16bf16_1k(__builtin_bit_cast(s16x4, a), __builtin_bit_cast(s16x4, b), c, 0, 0, 0);
}
DI u32 pk2(float a, float b) { f32x2 v = {a, b}; return __builtin_bit_cast(u32, __builtin_convertvector(v, bf16x2)); }
DI float lo16(u32 p) { return __uint_as_float(p << 16); }
DI float hi16(u32 p) { return __uint_as_float(p & 0xffff0000u); }
DI float bfr(float a) { return lo16(pk2(a, a)); }
DI u32x2 trd(u32 addr) {
  return __builtin_bit_cast(u32x2, __builtin_amdgcn_ds_read_tr16_b64_v4i16((LAS s16x4*)addr));
}
DI u32x4 cat(u32x2 a, u32x2 b) { return u32x4{a.x, a.y, b.x, b.y}; }
DI u32x4 ldsr128(u32 addr) { return *(LAS u32x4*)addr; }
DI void ldsw128(u32 addr, u32x4 v) { *(LAS u32x4*)addr = v; }
DI rsrc_t mkrs(const void* p, u32 bytes) { return __builtin_amdgcn_make_buffer_rsrc((void*)p, 0, bytes, 0x00020000); }
DI u32x4 ld128c(rsrc_t r, u32 vo, u32 so) { return __builtin_amdgcn_raw_buffer_load_b128(r, vo, so, 16); }
DI void st128c(rsrc_t r, u32 vo, u32x4 v) { __builtin_amdgcn_raw_buffer_store_b128(v, r, vo, 0, 16); }
DI void glds(rsrc_t r, u32 vo, u32 so, u32 lds) {
  asm volatile("s_mov_b32 m0, %3\n\ts_nop 0\n\tbuffer_load_dwordx4 %0, %1, %2 offen lds" ::"v"(vo), "s"(r), "s"(so), "s"(lds) : "memory");
}
#define VMCNT(n) asm volatile("s_waitcnt vmcnt(%0)" ::"i"(n) : "memory")
#define BARRIER() asm volatile("s_waitcnt lgkmcnt(0)\n\ts_barrier" ::: "memory")
template <int CTRL> DI float dppf(float v) { return __uint_as_float(__builtin_amdgcn_update_dpp(0u, __float_as_uint(v), CTRL, 0xf, 0xf, true)); }
DI float sum16(float v) { v += dppf<0xB1>(v); v += dppf<0x4E>(v); v += dppf<0x141>(v); v += dppf<0x140>(v); return v; }
DI float xsum(float v) {
  auto a = __builtin_amdgcn_permlane16_swap(__float_as_uint(v), __float_as_uint(v), false, false);
  v = __uint_as_float(a[0]) + __uint_as_float(a[1]);
  auto b = __builtin_amdgcn_permlane32_swap(__float_as_uint(v), __float_as_uint(v), false, false);
  return __uint_as_float(b[0]) + __uint_as_float(b[1]);
}
DI float xmax(float v) {
  auto a = __builtin_amdgcn_permlane16_swap(__float_as_uint(v), __float_as_uint(v), false, false);
  v = fmaxf(__uint_as_float(a[0]), __uint_as_float(a[1]));
  auto b = __builtin_amdgcn_permlane32_swap(__float_as_uint(v), __float_as_uint(v), false, false);
  return fmaxf(__uint_as_float(b[0]), __uint_as_float(b[1]));
}
DI float dot2(u32 a, u32 b, float c) { return __builtin_amdgcn_fdot2_f32_bf16(__builtin_bit_cast(bf16x2, a), __builtin_bit_cast(bf16x2, b), c, false); }
DI float sum32(float v) {
  v = sum16(v);
  auto a = __builtin_amdgcn_permlane16_swap(__float_as_uint(v), __float_as_uint(v), false, false);
  return __uint_as_float(a[0]) + __uint_as_float(a[1]);
}
#define SB() __builtin_amdgcn_sched_barrier(0)
DI u32 opq(u32 v) { asm volatile("" : "+s"(v)); return v; }
DI u32 opv(u32 v) { asm volatile("" : "+v"(v)); return v; }
#define PIN(x) asm volatile("" : "+v"(x))
template <int V> struct IC { static constexpr int v = V; };
template <int I, int N, class Fn> DI void sfor(Fn&& f) { if constexpr (I < N) { f(IC<I>{}); sfor<I + 1, N>(f); } }
DI u32 cw_ld(u32 addr) {
  u32 v; asm volatile("ds_read_b32 %0, %1\n\ts_waitcnt lgkmcnt(0)" : "=v"(v) : "v"(addr) : "memory");
  return __builtin_amdgcn_readfirstlane(v);
}
DI u32 cw_min4(u32 addr) {
  u32x4 v; asm volatile("ds_read_b128 %0, %1\n\ts_waitcnt lgkmcnt(0)" : "=v"(v) : "v"(addr) : "memory");
  return __builtin_amdgcn_readfirstlane(min(min(v.x, v.y), min(v.z, v.w)));
}
DI void cw_st(u32 addr, u32 val, int lane) {
  if (lane == 0) asm volatile("ds_write_b32 %0, %1" ::"v"(addr), "v"(val) : "memory");
}
DI void wait_min4(u32 addr, u32 need) {
#pragma unroll 1
  for (u32 sp = 0; sp < SPIN_L; ++sp) { if (cw_min4(addr) >= need) break; __builtin_amdgcn_s_sleep(1); }
}
DI void wait_word(u32 addr, u32 need) {
#pragma unroll 1
  for (u32 sp = 0; sp < SPIN_L; ++sp) { if (cw_ld(addr) >= need) break; __builtin_amdgcn_s_sleep(1); }
}
DI u32 cw_add(u32 addr, int lane) {
  u32 old = 0;
  if (lane == 0) asm volatile("ds_add_rtn_u32 %0, %1, %2\n\ts_waitcnt lgkmcnt(0)" : "=v"(old) : "v"(addr), "v"(1u) : "memory");
  return __builtin_amdgcn_readfirstlane(old);
}
DI void flag_set(char* ws, u32 base, int id, u64 tg, int lane) {
  if (lane < 8) __hip_atomic_store((GAS u64*)(ws + base + (lane * 256 + id) * 16), tg, __ATOMIC_RELAXED, __HIP_MEMORY_SCOPE_AGENT);
}
template <int N> DI void flag_wait(char* ws, u32 base, int rep, int id0, int id1, u64 tag) {
  GAS u64* p0 = (GAS u64*)(ws + base + (rep * 256 + id0) * 16);
  GAS u64* p1 = (GAS u64*)(ws + base + (rep * 256 + id1) * 16);
#pragma unroll 1
  for (u32 sp = 0; sp < SPIN_G; ++sp) {
    bool ok = __hip_atomic_load(p0, __ATOMIC_RELAXED, __HIP_MEMORY_SCOPE_AGENT) == tag;
    if (N > 1) ok &= __hip_atomic_load(p1, __ATOMIC_RELAXED, __HIP_MEMORY_SCOPE_AGENT) == tag;
    if (__all(ok)) break;
    __builtin_amdgcn_s_sleep(2);
  }
  __builtin_amdgcn_fence(__ATOMIC_ACQUIRE, "wavefront");
  asm volatile("" ::: "memory");
}
DI void publish(char* ws, u32 base, int id, u64 tag, u32 cnt, u32 nw, int lane) {
  VMCNT(0);
  if (cw_add(cnt, lane) == nw - 1) flag_set(ws, base, id, tag, lane);
}

#define NSTAMP 64
#define ST(i) do { u64 t_; asm volatile("s_memrealtime %0\n\ts_waitcnt lgkmcnt(0)" : "=s"(t_) :: "memory"); u32 t2_ = (u32)t_ | 1u; asm volatile("v_writelane_b32 %0, %1, %2" : "+v"(stampv) : "s"(t2_), "i"(i)); } while (0)

struct Ctx {
  const u16 *x, *wqkv, *wphi, *wo, *w1, *b1, *w2, *b2;
  u16* out;
  char* ws;
};

DI u32x4 hedgehog(f32x4 p) {
  const float L2E = 1.4426950408889634f;
  u32 a = pk2(p[0], p[1]), b = pk2(p[2], p[3]);
  float p0 = lo16(a) * L2E, p1 = hi16(a) * L2E, p2 = lo16(b) * L2E, p3 = hi16(b) * L2E;
  float mx = xmax(fmaxf(fmaxf(p0, p1), fmaxf(p2, p3)));
  float mn = -xmax(-fminf(fminf(p0, p1), fminf(p2, p3)));
  float e0 = __builtin_amdgcn_exp2f(p0 - mx), e1 = __builtin_amdgcn_exp2f(p1 - mx), e2 = __builtin_amdgcn_exp2f(p2 - mx), e3 = __builtin_amdgcn_exp2f(p3 - mx);
  float n0 = __builtin_amdgcn_exp2f(mn - p0), n1 = __builtin_amdgcn_exp2f(mn - p1), n2 = __builtin_amdgcn_exp2f(mn - p2), n3 = __builtin_amdgcn_exp2f(mn - p3);
  float rs = __builtin_amdgcn_rcpf(xsum((e0 + e1) + (e2 + e3)));
  float rn = __builtin_amdgcn_rcpf(xsum((n0 + n1) + (n2 + n3)));
  return u32x4{pk2(e0 * rs, e1 * rs), pk2(e2 * rs, e3 * rs), pk2(n0 * rn, n1 * rn), pk2(n2 * rn, n3 * rn)};
}

constexpr int c_need(int n) { return n < 9 ? (n == 0 ? 44 : n <= 2 ? 48 : n == 3 ? 52 : n <= 5 ? 56 : n == 6 ? 60 : 64) : 64 + 4 * (n - 8); }

__global__ __launch_bounds__(512, 2) void fused(const u16* __restrict__ x, const u16* __restrict__ wqkv, const u16* __restrict__ wphi,
                                                const u16* __restrict__ wo, const u16* __restrict__ w1, const u16* __restrict__ b1,
                                                const u16* __restrict__ w2, const u16* __restrict__ b2, u16* __restrict__ out, char* ws) {
#if __HIP_DEVICE_COMPILE__
  u32 stampv = 0;
  ST(0);
  const int tid = threadIdx.x, lane = tid & 63, w = __builtin_amdgcn_readfirstlane(tid >> 6);
  const int l15 = lane & 15, g = lane >> 4;
  const int bid = blockIdx.x, xc = bid & 7, r = bid >> 3;
  const bool isG = w < 4;
  const int hw = w & 3;
  const int stile = 8 * (xc >> 1) + (r & 7), dtile = 4 * (xc & 1) + (r >> 3);
  const bool roleA = r < 16;
  const int j = r & 15;
  const int b_dt = 4 * (xc & 1) + (j & 3), b_ft = 4 * (xc >> 1) + (j >> 2);
  const bool roleB = !roleA && b_ft < 15;
  const int a_rb = 4 * (xc >> 1) + (j & 3), a_grp = 4 * (xc & 1) + (j >> 2);
  const rsrc_t rws = mkrs(ws, WS_END);
  const rsrc_t rx = mkrs(x, S * D * 2);

  const u64 epoch = __hip_atomic_load((GAS u64*)(ws + WS_EPOCH), __ATOMIC_RELAXED, __HIP_MEMORY_SCOPE_AGENT);
#define tag (epoch + 1)
  if (tid < 64) *(LAS u32*)(L_CTL + tid * 4) = 0;
  BARRIER();
  ST(12);

  const u32 cswz = (((lane & 7) ^ (((lane >> 4) & 3) << 1)) << 4);
  const int crot = CROT;
  auto c_issue = [&](int n) {
    const u32 dst = ((6 + n) % 9) * 16384 + hw * 4096;
    if (n < 11) {
      const rsrc_t rw = mkrs(w2, F * S * 2);
      int ks = 4 * n + hw; if (ks > 42) ks = 42;
      ks += crot; if (ks >= 43) ks -= 43;
      const u32 vo = (lane >> 3) * 4096 + cswz + stile * 128;
#pragma unroll
      for (int m = 0; m < 4; ++m) glds(rw, vo, ks * 131072 + m * 32768, dst + m * 1024);
    } else {
      const rsrc_t rw = mkrs(wo, D * D * 2);
      const int kt = 4 * (n - 11) + hw;
      const u32 vo = (lane >> 3) * 1024 + cswz + dtile * 128;
#pragma unroll
      for (int m = 0; m < 4; ++m) glds(rw, vo, kt * 32768 + m * 8192, dst + m * 1024);
    }
  };
  int c_first = 0;
  u32 fullv = 0;
  auto pub = [&](u32 v) { if (v > fullv) { fullv = v; cw_st(L_FULL + hw * 4, v, lane); } };

  if (roleA) {
    const int rq = w & 3, hp = w >> 2;
    const rsrc_t rw = mkrs(wqkv, D * 3 * D * 2);
    const int row0 = a_rb * 128;
    const u32 xvo = (row0 + 16 * w + (lane >> 3)) * 1024, xsw = lane >> 4;
    const u32 wvo = (lane >> 3) * 3072 + (((lane & 7) ^ ((((lane >> 4) & 1) | ((w & 1) << 1)) << 1)) << 4) + a_grp * 128;
    const u32 xv0 = xvo + (((lane & 7) ^ xsw) << 4), xv1 = xvo + (((lane & 7) ^ (xsw + 4)) << 4);
    auto a_issue1 = [&](int c, int i) {
      const u32 dst = (c % 3) * 40960;
      if (i < 2) glds(rx, i ? xv1 : xv0, i * 8192 + c * 128, dst + w * 2048 + i * 1024);
      else glds(rw, wvo, (64 * c + 8 * w) * 3072 + (i - 2) * 1024, dst + 16384 + (i - 2) * 8192 + w * 1024);
    };
    u32 wpr[2][4];
#pragma unroll
    for (int e = 0; e < 2; ++e) {
      const u16* p = wphi + (a_grp * 4 + 2 * hp + e) * 256 + 4 * g * 16 + l15;
#pragma unroll
      for (int i = 0; i < 4; ++i) wpr[e][i] = p[16 * i];
    }
    SB();
#pragma unroll
    for (int i = 0; i < 10; ++i) a_issue1(i / 5, i % 5);
    ST(13);
    f32x4 aq[2][2], ak[2][2], av[2][2];
#pragma unroll
    for (int a = 0; a < 2; ++a)
#pragma unroll
      for (int e = 0; e < 2; ++e) { aq[a][e] = f32x4{0, 0, 0, 0}; ak[a][e] = aq[a][e]; av[a][e] = aq[a][e]; }
    const u32 xb0 = (32 * rq + l15) * 128 + ((g ^ ((l15 >> 1) & 7)) << 4);
    const u32 wf = ((l15 >> 3) & 1) | ((g & 1) << 1);
    u32 wb[2];
#pragma unroll
    for (int e = 0; e < 2; ++e) wb[e] = 16384 + (8 * g + (l15 >> 2)) * 128 + (((2 * hp + e) ^ wf) << 5) + (l15 & 3) * 8;
    u32x4 xa[2][2], wa[2][6];
    auto a_step = [&](bool mma, int kc, bool rd, int kr, u32 sb, int cn, int p0, int np) {
      u32 xb = 0, wbb[2] = {0, 0};
      if (rd) { xb = opv(sb + (xb0 ^ (kr * 64))); wbb[0] = opv(sb + wb[0]); wbb[1] = opv(sb + wb[1]); }
#pragma unroll
      for (int q = 0; q < 6; ++q) {
        const int e = q / 3, sec = q % 3;
        if (rd) {
          if (q < 2) xa[kr][q] = ldsr128(xb + q * 2048);
          const u32 ad = wbb[e] + sec * 8192 + kr * 4096;
          wa[kr][q] = cat(trd(ad), trd(ad + 512));
        }
        SB();
        if (mma) {
#pragma unroll
          for (int a = 0; a < 2; ++a) {
            if (sec == 0) { aq[a][e] = mfma32(wa[kc][q], xa[kc][a], aq[a][e]); PIN(aq[a][e]); }
            if (sec == 1) { ak[a][e] = mfma32(wa[kc][q], xa[kc][a], ak[a][e]); PIN(ak[a][e]); }
            if (sec == 2) { av[a][e] = mfma32(xa[kc][a], wa[kc][q], av[a][e]); PIN(av[a][e]); }
          }
        }
        if ((q & 1) && q / 2 < np) a_issue1(cn, p0 + q / 2);
        SB();
      }
    };
    u64 early = 0;
    GAS u64* const pfl = (GAS u64*)(ws + (lane < 8 ? WS_FH + (xc * 256 + a_rb * 8 + lane) * 16 : WS_FCS + (xc * 256 + a_grp) * 16));
    u32x2 stl[2]; u32x4 stv[2][2], csq[2], csk[2]; u32 csv[2];
#pragma unroll
    for (int c = 0; c < 8; ++c) {
      if (c == 7 && w == 0) {
        VMCNT(0);
        asm volatile("" : "+v"(early));
        if (!__all(early == tag)) flag_wait<1>(ws, lane < 8 ? WS_FH : WS_FCS, xc, lane < 8 ? a_rb * 8 + lane : a_grp, 0, tag);
      }
      if (c < 7) VMCNT(5); else VMCNT(0);
      BARRIER();
      ST(16 + c);
      const u32 sb = (c % 3) * 40960;
      if (c == 7) {
#pragma unroll
        for (int a = 0; a < 2; ++a) {
          const u32 rb8 = WS_STAT + (row0 + 32 * rq + 16 * a) * 8;
          stl[a] = __builtin_amdgcn_raw_buffer_load_b64(rws, rb8 + l15 * 8, 0, 16);
          stv[a][0] = ld128c(rws, rb8 + g * 32, 0);
          stv[a][1] = ld128c(rws, rb8 + g * 32 + 16, 0);
        }
#pragma unroll
        for (int e = 0; e < 2; ++e) {
          const u32 cb = WS_CS + (a_grp * 64 + (2 * hp + e) * 16) * 4;
          csq[e] = ld128c(rws, cb + g * 16, 0);
          csk[e] = ld128c(rws, cb + 2048 + g * 16, 0);
          csv[e] = __builtin_amdgcn_raw_buffer_load_b32(rws, cb + 4096 + l15 * 4, 0, 16);
        }
      }
      if (c == 6 && w == 0) asm volatile("global_load_dwordx2 %0, %1, off sc1" : "=v"(early) : "v"(pfl) : "memory");
      a_step(c > 0, 1, true, 0, sb, c + 2, 0, c < 6 ? 2 : 0);
      a_step(true, 0, true, 1, sb, c + 2, 2, c < 6 ? 3 : 0);
    }
    a_step(true, 1, false, 0, 0, 0, 0, 0);
    u32x2 wph[2];
#pragma unroll
    for (int e = 0; e < 2; ++e) wph[e] = u32x2{wpr[e][0] | (wpr[e][1] << 16), wpr[e][2] | (wpr[e][3] << 16)};
    ST(2);
    BARRIER();
    const u32 one2 = 0x3f803f80u;
    u32x4 selP = {0, 0, 0, 0}, selN = {0, 0, 0, 0};
    {
      const int dlt = l15 - 4 * g;
      const u32 v0 = dlt == 0 ? 0x3f80u : dlt == 1 ? 0x3f800000u : 0u, v1 = dlt == 2 ? 0x3f80u : dlt == 3 ? 0x3f800000u : 0u;
      selP.x = v0; selP.y = v1; selN.z = v0; selN.w = v1;
    }
    auto fold = [&](f32x4 acc, float m, float ri, u32x4 cs) {
      return u32x2{pk2((acc[0] - m * __uint_as_float(cs.x)) * ri, (acc[1] - m * __uint_as_float(cs.y)) * ri),
                   pk2((acc[2] - m * __uint_as_float(cs.z)) * ri, (acc[3] - m * __uint_as_float(cs.w)) * ri)};
    };
    const f32x4 zero4 = {0, 0, 0, 0};
#pragma unroll
    for (int e = 0; e < 2; ++e) {
      u32x2 tp[2], tn[2], vb[2];
#pragma unroll
      for (int a = 0; a < 2; ++a) {
        const u32x4 ph = hedgehog(mfma16(wph[e], fold(ak[a][e], __uint_as_float(stl[a].x), __uint_as_float(stl[a].y), csk[e]), zero4));
        const f32x4 p4 = mfma32(ph, selP, zero4), n4 = mfma32(ph, selN, zero4);
        tp[a] = u32x2{pk2(p4[0], p4[1]), pk2(p4[2], p4[3])};
        tn[a] = u32x2{pk2(n4[0], n4[1]), pk2(n4[2], n4[3])};
        const float cv = __uint_as_float(csv[e]);
        const f32x4 t = av[a][e];
        vb[a] = u32x2{pk2((t[0] - __uint_as_float(stv[a][0].x) * cv) * __uint_as_float(stv[a][0].y), (t[1] - __uint_as_float(stv[a][0].z) * cv) * __uint_as_float(stv[a][0].w)),
                      pk2((t[2] - __uint_as_float(stv[a][1].x) * cv) * __uint_as_float(stv[a][1].y), (t[3] - __uint_as_float(stv[a][1].z) * cv) * __uint_as_float(stv[a][1].w))};
      }
      const u32x4 P = cat(tp[0], tp[1]), N = cat(tn[0], tn[1]), V = cat(vb[0], vb[1]), ones = {one2, one2, one2, one2};
      const f32x4 kvp = mfma32(P, V, zero4), kvn = mfma32(N, V, zero4), zp = mfma32(P, ones, zero4), zn = mfma32(N, ones, zero4);
      const int ti = (2 * hp + e) * 2;
      ldsw128(L_AKV + (ti * 4 + rq) * 1024 + lane * 16, __builtin_bit_cast(u32x4, kvp));
      ldsw128(L_AKV + ((ti + 1) * 4 + rq) * 1024 + lane * 16, __builtin_bit_cast(u32x4, kvn));
      if (l15 == 0) {
        ldsw128(L_AZ + (ti * 4 + rq) * 64 + g * 16, __builtin_bit_cast(u32x4, zp));
        ldsw128(L_AZ + ((ti + 1) * 4 + rq) * 64 + g * 16, __builtin_bit_cast(u32x4, zn));
      }
    }
    BARRIER();
    ST(3);
    if (isG) {
      f32x4 sp = zero4, sn = zero4;
      float zz = 0.f;
#pragma unroll
      for (int q = 0; q < 4; ++q) {
        sp += __builtin_bit_cast(f32x4, ldsr128(L_AKV + (w * 8 + q) * 1024 + lane * 16));
        sn += __builtin_bit_cast(f32x4, ldsr128(L_AKV + (w * 8 + 4 + q) * 1024 + lane * 16));
        zz += *(LAS float*)(L_AZ + (w * 8 + ((lane >> 4) & 1) * 4 + q) * 64 + l15 * 4);
      }
      const u32 rec = a_rb * 32 + a_grp * 4 + w;
      st128c(rws, WS_KV + rec * 1024 + lane * 16, u32x4{pk2(sp[0], sp[1]), pk2(sp[2], sp[3]), pk2(sn[0], sn[1]), pk2(sn[2], sn[3])});
      if (lane < 32) __builtin_amdgcn_raw_buffer_store_b32(__float_as_uint(zz), rws, WS_Z + rec * 128 + lane * 4, 0, 16);
      publish(ws, WS_FKV, a_rb * 8 + a_grp, tag, L_CNT, 4, lane);
    } else {
#pragma unroll
      for (int n = 0; n < 6; ++n) c_issue(n);
      c_first = 6;
    }
    ST(4);
    u32x4 phq[2][2];
#pragma unroll
    for (int a = 0; a < 2; ++a)
#pragma unroll
      for (int e = 0; e < 2; ++e)
        phq[a][e] = hedgehog(mfma16(wph[e], fold(aq[a][e], __uint_as_float(stl[a].x), __uint_as_float(stl[a].y), csq[e]), zero4));
    ST(5);
    if (isG) {
      if (w == 0) { flag_wait<1>(ws, WS_FKV, xc, (lane & 15) * 8 + a_grp, 0, tag); cw_st(L_LND, 1, lane); }
      else wait_word(L_LND, 1);
      ST(6);
      u32x4 kvl[16]; u32 zl[16];
#pragma unroll
      for (int q = 0; q < 16; ++q) {
        const u32 rec = q * 32 + a_grp * 4 + w;
        kvl[q] = ld128c(rws, WS_KV + lane * 16, rec * 1024);
        zl[q] = __builtin_amdgcn_raw_buffer_load_b32(rws, WS_Z + (lane & 31) * 4, rec * 128, 16);
      }
      SB();
      const u32 selo = opv(0x3f80u), sehi = opv(0x3f800000u);
      float a8[8], zz = 0.f;
#pragma unroll
      for (int i = 0; i < 8; ++i) a8[i] = 0.f;
#pragma unroll
      for (int q = 0; q < 16; ++q) {
#pragma unroll
        for (int i = 0; i < 4; ++i) { a8[2 * i] = dot2(kvl[q][i], selo, a8[2 * i]); a8[2 * i + 1] = dot2(kvl[q][i], sehi, a8[2 * i + 1]); }
        zz += __uint_as_float(zl[q]);
      }
      ldsw128(L_AT + w * 1024 + lane * 16, u32x4{pk2(a8[0], a8[1]), pk2(a8[2], a8[3]), pk2(a8[4], a8[5]), pk2(a8[6], a8[7])});
      if (lane < 32) *(LAS u16*)(L_AZT + w * 64 + lane * 2) = (u16)pk2(zz, zz);
    }
    BARRIER();
    ST(14);
    {
      u32x4 akv[2], az[2];
#pragma unroll
      for (int e = 0; e < 2; ++e) {
        akv[e] = ldsr128(L_AT + (2 * hp + e) * 1024 + lane * 16);
        az[e] = cat(*(LAS u32x2*)(L_AZT + (2 * hp + e) * 64 + g * 8), *(LAS u32x2*)(L_AZT + (2 * hp + e) * 64 + 32 + g * 8));
      }
#pragma unroll
      for (int a = 0; a < 2; ++a) {
        u32x2 ov[2];
#pragma unroll
        for (int e = 0; e < 2; ++e) {
          const f32x4 num = mfma32(akv[e], phq[a][e], zero4), den = mfma32(az[e], phq[a][e], zero4);
          const float rd = __builtin_amdgcn_rcpf(bfr(bfr(den[0]) + 1e-6f));
          const u32 n01 = pk2(num[0], num[1]), n23 = pk2(num[2], num[3]);
          ov[e] = u32x2{pk2(lo16(n01) * rd, hi16(n01) * rd), pk2(lo16(n23) * rd, hi16(n23) * rd)};
        }
        st128c(rws, WS_O + ((a_grp * 2 + hp) * 128 + a_rb * 8 + 2 * rq + a) * 1024 + lane * 16, cat(ov[0], ov[1]));
      }
      publish(ws, WS_FO, a_rb * 8 + a_grp, tag, L_CNT + 4, 8, lane);
      if (!isG) pub(16 + 6);
    }
    BARRIER();
    if (isG) cw_st(L_PROG + hw * 4, 64, lane);
    ST(7);
  } else {
    const int li = xc * 16 + j;
    const int rot = BROT;
    const rsrc_t rw1 = mkrs(w1, S * F * 2);
    u32 vo[6];
    u32x4 cst[24];
    auto b_issue = [&](int m) {
      const u32 so = ((4 * m + hw + rot) & 63) * (32 * F * 2);
#pragma unroll
      for (int n = 0; n < 6; ++n) glds(rw1, vo[n], so, (m % 6) * 24576 + hw * 6144 + n * 1024);
    };
    {
      const int rl = 2 * w + (lane >> 5), row = li * 16 + rl;
      u32x4 xr[2];
      asm volatile("buffer_load_dwordx4 %0, %2, %3, 0 offen\n\tbuffer_load_dwordx4 %1, %2, %3, 0 offen offset:512"
                   : "=&v"(xr[0]), "=&v"(xr[1]) : "v"(row * 1024 + (lane & 31) * 16), "s"(rx) : "memory");
      if (!isG && roleB) {
#pragma unroll
        for (int n = 0; n < 6; ++n) {
          const int c = n * 64 + lane, rr = c / 12;
          int ch = (c % 12) ^ (((rr >> 3) & 1) << 1);
          if (b_ft == 14) ch &= 3;
          vo[n] = rr * (F * 2) + ch * 16 + b_ft * 192;
        }
        b_issue(0);
        ST(13);
        VMCNT(6);
      } else if (!roleB) {
        const rsrc_t rw = mkrs(wqkv, D * 3 * D * 2);
#pragma unroll
        for (int i = 0; i < 24; ++i)
          cst[i] = __builtin_amdgcn_raw_buffer_load_b128(rw, (w * 64 + (lane >> 3)) * 3072 + ((xc & 1) * 4 + (j & 3)) * 128 + (lane & 7) * 16, (i / 3) * 8 * 3072 + (i % 3) * 1024, 0);
        SB();
        VMCNT(24);
      } else {
        ST(13);
        VMCNT(0);
      }
      asm volatile("" : "+v"(xr[0]), "+v"(xr[1]));
      ST(1);
      u32 pk[8] = {xr[0].x, xr[0].y, xr[0].z, xr[0].w, xr[1].x, xr[1].y, xr[1].z, xr[1].w};
#pragma unroll
      for (int pass = 0; pass < 2; ++pass) {
        float s1 = 0.f, s2 = 0.f;
#pragma unroll
        for (int i = 0; i < 8; ++i) { s1 = dot2(pk[i], 0x3f803f80u, s1); s2 = dot2(pk[i], pk[i], s2); }
        const float m = sum32(s1) * (1.f / D);
        const float var = bfr(fmaxf(sum32(s2) * (1.f / D) - m * m, 0.f));
        const float mb = bfr(m);
        const float ri = __builtin_amdgcn_rcpf(bfr(__builtin_amdgcn_sqrtf(bfr(var + 1e-5f))));
#pragma unroll
        for (int i = 0; i < 8; ++i) pk[i] = pk2((lo16(pk[i]) - mb) * ri, (hi16(pk[i]) - mb) * ri);
        if (pass == 0 && (lane & 31) == 0) *(LAS u32x2*)(L_LNS + rl * 8) = u32x2{__float_as_uint(mb), __float_as_uint(ri)};
      }
      const u32 wa = L_LN + rl * LN_PITCH + (rl >> 3) * 32 + (lane & 31) * 16;
      ldsw128(wa, u32x4{pk[0], pk[1], pk[2], pk[3]});
      ldsw128(wa + 512, u32x4{pk[4], pk[5], pk[6], pk[7]});
      cw_add(L_CNT + 8, lane);
      ST(14);
    }
    if (isG) {
      wait_word(L_CNT + 8, 8);
      ST(15);
#pragma unroll
      for (int rr = 0; rr < 4; ++rr) {
        const int item = w * 16 + rr * 4 + g, dt = item >> 1, oct = item & 1;
        const u32 ad = L_LN + (8 * oct + (l15 >> 2)) * LN_PITCH + oct * 32 + dt * 32 + (l15 & 3) * 8;
        const u32x4 hv = cat(trd(ad), trd(ad + 4 * LN_PITCH));
        st128c(rws, WS_H + (((li >> 1) * 32 + dt) * 4 + (li & 1) * 2 + oct) * 256 + l15 * 16, hv);
      }
      if (w == 0 && lane < 8) st128c(rws, WS_STAT + li * 128 + lane * 16, ldsr128(L_LNS + lane * 16));
      ST(2);
      VMCNT(0);
      if (cw_add(L_CNT + 12, lane) == 3) { flag_set(ws, WS_FH, li, tag, lane); cw_st(L_LND, 1, lane); }
      ST(3);
    }
    if (!roleB) {
      const int grp = (xc & 1) * 4 + (j & 3);
      const u32 sv = (l15 & 1) ? 0x3f800000u : 0x3f80u;
      const u32x4 sel = {l15 >> 1 == 0 ? sv : 0u, l15 >> 1 == 1 ? sv : 0u, l15 >> 1 == 2 ? sv : 0u, l15 >> 1 == 3 ? sv : 0u};
      f32x4 cs[3] = {{0, 0, 0, 0}, {0, 0, 0, 0}, {0, 0, 0, 0}};
#pragma unroll
      for (int i = 0; i < 24; ++i) cs[i % 3] = mfma32(cst[i], sel, cs[i % 3]);
#pragma unroll
      for (int sec = 0; sec < 3; ++sec) ldsw128((w * 3 + sec) * 1024 + lane * 16, __builtin_bit_cast(u32x4, cs[sec]));
      if (cw_add(L_CNT, lane) == 7) {
        const int pos = lane >> 3, jj = lane & 7;
        float o3[3];
#pragma unroll
        for (int sec = 0; sec < 3; ++sec) {
          float a = 0.f;
#pragma unroll
          for (int q = 0; q < 8; ++q) {
            const u32 ad = (q * 3 + sec) * 1024 + ((pos >> 2) * 16 + jj) * 16 + (pos & 3) * 4;
            a += *(LAS float*)ad + *(LAS float*)(ad + 512);
          }
          o3[sec] = a;
        }
#pragma unroll
        for (int sec = 0; sec < 3; ++sec) __builtin_amdgcn_raw_buffer_store_b32(__float_as_uint(o3[sec]), rws, WS_CS + sec * 2048 + grp * 256 + lane * 4, 0, 16);
        VMCNT(0);
        flag_set(ws, WS_FCS, grp, tag, lane);
        cw_st(L_CNT + 4, 1, lane);
        ST(4);
      }
    }
    if (isG) {
    if (roleB) {
      u32x2 bias[6];
#pragma unroll
      for (int t = 0; t < 6; ++t) { int f = b_ft * 96 + t * 16 + 4 * g; if (f > F - 4) f = F - 4; bias[t] = *(const GAS u32x2*)(b1 + f); }
      if (w == 0) {
        flag_wait<2>(ws, WS_FH, xc, lane, lane + 64, tag);
        cw_st(L_GO, 1, lane);
      } else {
        wait_word(L_GO, 1);
      }
      ST(4);
      const u32 hvo = (b_dt * 4 + w) * 1024 + lane * 16;
      constexpr int P = 16;
      u32x4 hr[P];
#pragma unroll
      for (int i = 0; i < P; ++i) hr[i] = ld128c(rws, hvo, WS_H + ((i + rot) & 63) * 32768);
      __builtin_amdgcn_sched_barrier(0);
      f32x4 acc[6];
#pragma unroll
      for (int t = 0; t < 6; ++t) acc[t] = f32x4{0, 0, 0, 0};
      const u32 rowb = (8 * g + (l15 >> 2)) * 192 + (l15 & 3) * 8;
      const u32 bE = rowb + (g & 1) * 32, bO = rowb - (g & 1) * 32;
      u32x4 wfr[2][6];
      u32 avail = 0;
      auto b_need = [&](u32 need) { if (avail < need) { wait_min4(L_FULL, need); avail = cw_min4(L_FULL); } };
      u32 gE = bE, gO = bO;
      auto b_rd = [&](int buf, int k, int t) {
        const u32 ad = ((t & 1) ? gO : gE) + k * 6144 + t * 32;
        wfr[buf][t] = cat(trd(ad), trd(ad + 768));
      };
      b_need(1);
#pragma unroll
      for (int t = 0; t < 6; ++t) b_rd(0, 0, t);
      u32 pos = 0;
#pragma unroll 1
      for (int mo = 0; mo < 4; ++mo) {
        const u32 hv2 = mo < 3 ? hvo : 0x80000000u;
#pragma unroll
        for (int mi = 0; mi < 4; ++mi) {
          const int m = mo * 4 + mi;
          ST(16 + mi);
#pragma unroll
          for (int k = 0; k < 4; ++k) {
            if (k == 3) {
              cw_st(L_PROG + w * 4, 4 * (m + 1), lane);
              pos = pos == 5 ? 0 : pos + 1; gE = opv(bE + pos * 24576); gO = opv(bO + pos * 24576);
              b_need(m < 15 ? m + 2 : 0);
            }
#pragma unroll
            for (int t = 0; t < 6; ++t) {
              b_rd((k + 1) & 1, (k + 1) & 3, t);
              SB();
              acc[t] = mfma32(wfr[k & 1][t], hr[mi * 4 + k], acc[t]);
              PIN(acc[t]);
              SB();
            }
            hr[mi * 4 + k] = ld128c(rws, hv2, WS_H + ((m * 4 + k + P + rot) & 63) * 32768);
            SB();
          }
        }
      }
      ST(5);
#pragma unroll
      for (int m3 = 0; m3 < 3; ++m3) {
        u32x4 uv;
#pragma unroll
        for (int h2 = 0; h2 < 2; ++h2) {
          const int t = 2 * m3 + h2;
          float o4[4];
#pragma unroll
          for (int i = 0; i < 4; ++i) {
            const u32 bw = i < 2 ? bias[t].x : bias[t].y;
            const float v = acc[t][i] + ((i & 1) ? hi16(bw) : lo16(bw));
            const float zz = v * (1.f + 0.044715f * v * v) * (-2.f * 0.7978845608028654f * 1.4426950408889634f);
            o4[i] = v * __builtin_amdgcn_rcpf(1.f + __builtin_amdgcn_exp2f(zz));
          }
          uv[2 * h2] = pk2(o4[0], o4[1]); uv[2 * h2 + 1] = pk2(o4[2], o4[3]);
        }
        if (b_ft * 3 + m3 < 43) st128c(rws, WS_U + ((b_ft * 3 + m3) * 32 + b_dt * 4 + w) * 1024 + lane * 16, uv);
      }
      ST(6);
      publish(ws, WS_FU, b_dt * 16 + b_ft, tag, L_CNT, 4, lane);
      ST(7);
    } else {
      wait_word(L_LND, 1);
      cw_st(L_PROG + w * 4, 64, lane);
    }
    } else if (roleB) {
#pragma unroll
    for (int m = 1; m < 16; ++m) {
      if (m == 5) wait_word(L_LND, 1);
      if (m >= 6) {
        if (cw_min4(L_PROG) < 4 * (m - 5)) { VMCNT(0); pub(m); wait_min4(L_PROG, 4 * (m - 5)); }
      }
      b_issue(m);
      if (m >= 3) { VMCNT(18); pub(m - 2); }
      ST(16 + m);
    }
    VMCNT(12); pub(14); VMCNT(6); pub(15); VMCNT(0); pub(16);
    ST(4);
  } else {
    wait_word(L_CNT + 4, 1);
    }
  }

  if (isG) {
    const int srow = stile * 64 + l15;
    u32x2 xin[4]; u32 b2v[4];
#pragma unroll
    for (int t = 0; t < 4; ++t) {
      xin[t] = *(const GAS u32x2*)(x + (srow + 16 * t) * D + dtile * 64 + w * 16 + 4 * g);
      b2v[t] = b2[srow + 16 * t];
    }
    if (w == 0) {
      flag_wait<1>(ws, WS_FU, xc, dtile * 16 + (l15 < 15 ? l15 : 0), 0, tag);
      cw_st(L_GO, 2, lane);
    } else {
      wait_word(L_GO, 2);
    }
    ST(8);
    constexpr int P = 16;
    const u32 uvo = (dtile * 4 + w) * 1024 + lane * 16, ovo = (stile * 4 + w) * 1024 + lane * 16;
    auto uoff = [&](int i) { int ks = i + crot; if (ks >= 43) ks -= 43; return WS_U + ks * 32768; };
    u32x4 ar[P];
#pragma unroll
    for (int i = 0; i < P; ++i) ar[i] = ld128c(rws, uvo, uoff(i));
    __builtin_amdgcn_sched_barrier(0);
    f32x4 ay[4], as[4];
#pragma unroll
    for (int t = 0; t < 4; ++t) { ay[t] = f32x4{0, 0, 0, 0}; as[t] = ay[t]; }
    u32 tb[4];
    {
      const int row = 4 * g + (l15 >> 2), f = (row >> 1) & 3;
#pragma unroll
      for (int t = 0; t < 4; ++t) tb[t] = row * 128 + ((t ^ f) << 5) + (l15 & 3) * 8;
    }
    u32x4 wfr[3][4];
    u32 avail = 0;
    auto c_needf = [&](u32 need) { if (avail < need) { wait_min4(L_FULL, need); avail = cw_min4(L_FULL); } };
    u32 tg[4];
    auto c_pre = [&](auto ic) {
      constexpr int i = decltype(ic)::v;
      if (i < 60 && (i & 3) == 0) {
        c_needf(16 + i / 4 + 1);
#pragma unroll
        for (int t = 0; t < 4; ++t) tg[t] = opv(tb[t] + ((6 + i / 4) % 9) * 16384);
      }
    };
    auto c_rd = [&](auto ic, int t) {
      constexpr int i = decltype(ic)::v;
      if (i == 43 || i >= 60) return;
      const u32 ad = tg[t] + (i & 3) * 4096;
      wfr[i % 3][t] = cat(trd(ad), trd(ad + 2048));
    };
    c_pre(IC<0>{});
#pragma unroll
    for (int t = 0; t < 4; ++t) { c_rd(IC<0>{}, t); }
#pragma unroll
    for (int t = 0; t < 4; ++t) { c_rd(IC<1>{}, t); }
    sfor<0, 60>([&](auto ic) {
      constexpr int i = decltype(ic)::v;
      if ((i & 3) == 0) ST(32 + i / 4);
      if (i == 36) {
        wait_word(L_GOO, 1);
        ST(9);
#pragma unroll
        for (int q = 44; q < 52; ++q) ar[q % P] = ld128c(rws, ovo, WS_O + (q - 44) * 131072);
      }
      c_pre(IC<i + 2>{});
#pragma unroll
      for (int t = 0; t < 4; ++t) {
        c_rd(IC<i + 2>{}, t);
        SB();
        if (i < 43) { ay[t] = mfma32(ar[i % P], wfr[i % 3][t], ay[t]); PIN(ay[t]); }
        if (i > 43) { as[t] = mfma32(wfr[i % 3][t], ar[i % P], as[t]); PIN(as[t]); }
        SB();
      }
      if (((i + 2) & 3) == 3 && i + 2 < 60) cw_st(L_PROG + w * 4, 64 + 4 * ((i + 2) / 4 + 1), lane);
      constexpr int nx = i + P;
      if (nx < 43) ar[i % P] = ld128c(rws, uvo, uoff(nx));
      else if (nx >= 52 && nx < 60) ar[i % P] = ld128c(rws, ovo, WS_O + (nx - 44) * 131072);
      SB();
    });
    ST(10);
#pragma unroll
    for (int t = 0; t < 4; ++t)
      if (t != w) ldsw128(L_SCR + (t * 4 + w) * 1024 - (w > t ? 1024 : 0) - t * 1024 + lane * 16, __builtin_bit_cast(u32x4, as[t]));
    BARRIER();
#pragma unroll
    for (int t = 0; t < 4; ++t) {
      f32x4 sv;
      if (t == w) sv = w == 0 ? as[0] : w == 1 ? as[1] : w == 2 ? as[2] : as[3];
      else sv = __builtin_bit_cast(f32x4, ldsr128(L_SCR + (w * 3 + t - (t > w ? 1 : 0)) * 1024 + lane * 16));
      const float bb = lo16(b2v[t]);
      float o4[4];
#pragma unroll
      for (int i = 0; i < 4; ++i) {
        const float yy = ay[t][i] + bb;
        const float gg = yy * __builtin_amdgcn_rcpf(1.f + __builtin_amdgcn_exp2f(yy * -1.4426950408889634f));
        const u32 xw = i < 2 ? xin[t].x : xin[t].y;
        o4[i] = ((i & 1) ? hi16(xw) : lo16(xw)) + sv[i] * gg;
      }
      *(GAS u32x2*)(out + (srow + 16 * t) * D + dtile * 64 + w * 16 + 4 * g) = u32x2{pk2(o4[0], o4[1]), pk2(o4[2], o4[3])};
    }
  } else {
#pragma unroll
    for (int n = 0; n < 15; ++n) {
      if (n >= c_first) {
        if (cw_min4(L_PROG) < c_need(n)) { VMCNT(0); pub(16 + n); wait_min4(L_PROG, c_need(n)); }
        c_issue(n);
        VMCNT(12);
        pub(16 + n - 2);
        ST(32 + n);
      }
    }
    VMCNT(0);
    pub(31);
    ST(8);
    if (w == 4) {
      flag_wait<1>(ws, WS_FO, xc, (stile >> 1) * 8 + (lane & 7), 0, tag);
      cw_st(L_GOO, 1, lane);
      if (bid == 0 && lane == 0) __hip_atomic_store((GAS u64*)(ws + WS_EPOCH), tag, __ATOMIC_RELAXED, __HIP_MEMORY_SCOPE_AGENT);
    }
    ST(9);
    BARRIER();
  }

  if (bid == AMP_BID && w == AMP_W) {
    u32 dlt;
    dlt = 0;
    if (dlt > 5000) dlt = 5000;
    u64 t0 = __builtin_amdgcn_s_memrealtime();
    while (__builtin_amdgcn_s_memrealtime() - t0 < (u64)dlt * 20) {}
  }
#endif
}
#undef tag
}

extern "C" void kernel_launch(void* const* d_in, const int* in_sizes, int n_in, void* d_out, int out_size, void* d_ws, size_t ws_size,
                              hipStream_t stream) {
  using namespace hk;
  (void)hipFuncSetAttribute((const void*)fused, hipFuncAttributeMaxDynamicSharedMemorySize, LDS_BYTES);
  fused<<<256, 512, LDS_BYTES, stream>>>((const u16*)d_in[0], (const u16*)d_in[1], (const u16*)d_in[2], (const u16*)d_in[3], (const u16*)d_in[5],
                                         (const u16*)d_in[6], (const u16*)d_in[7], (const u16*)d_in[8], (u16*)d_out, (char*)d_ws);
}
```

```cpp
#define STAMPS
#define AMP 0
#define PREPOST
#define AMP_BID 128
#define AMP_W 0
#include <hip/hip_runtime.h>
#include <stdint.h>

namespace hk {
typedef unsigned short u16;
typedef unsigned u32;
typedef unsigned long long u64;
typedef u32 u32x4 __attribute__((ext_vector_type(4)));
typedef u32 u32x2 __attribute__((ext_vector_type(2)));
typedef float f32x4 __attribute__((ext_vector_type(4)));
typedef float f32x2 __attribute__((ext_vector_type(2)));
typedef __bf16 bf16x8 __attribute__((ext_vector_type(8)));
typedef __bf16 bf16x2 __attribute__((ext_vector_type(2)));
typedef short s16x4 __attribute__((ext_vector_type(4)));
#define LAS __attribute__((address_space(3)))
#define GAS __attribute__((address_space(1)))
#define DI __device__ __forceinline__
typedef __amdgpu_buffer_rsrc_t rsrc_t;

#define CROT 0
#define BROT 0
constexpr int S = 2048, D = 512, F = 1376;
constexpr u32 WS_EPOCH = 0;
constexpr u32 FL_SZ = 8 * 256 * 16;
constexpr u32 WS_FH = 4096, WS_FKV = WS_FH + FL_SZ, WS_FO = WS_FKV + FL_SZ, WS_FU = WS_FO + FL_SZ, WS_FCS = WS_FU + FL_SZ;
constexpr u32 WS_STAT = WS_FCS + FL_SZ;
constexpr u32 WS_CS = WS_STAT + S * 8;
constexpr u32 WS_Z = WS_CS + 8192;
constexpr u32 WS_KV = WS_Z + 16 * 32 * 128;
constexpr u32 WS_H = WS_KV + 16 * 32 * 1024;
constexpr u32 WS_O = WS_H + S * D * 2;
constexpr u32 WS_U = WS_O + S * D * 2;
constexpr u32 WS_END = WS_U + D * F * 2;
constexpr u32 WS_STAMP = 64u << 20;
constexpr u32 RING = 147456, L_SCR = RING, L_CTL = 163840 - 256, LDS_BYTES = 163840;
constexpr u32 L_FULL = L_CTL, L_PROG = L_CTL + 16, L_GO = L_CTL + 32, L_GOO = L_CTL + 36, L_LND = L_CTL + 40, L_CNT = L_CTL + 48;
constexpr u32 L_LN = RING - 2048, LN_PITCH = 1088, L_LNS = L_LN + 16 * LN_PITCH + 64;
constexpr u32 L_AKV = 49152, L_AZ = L_AKV + 32768, L_AT = L_AZ + 2048, L_AZT = L_AT + 4096;
constexpr u32 SPIN_G = 1u << 15, SPIN_L = 1u << 20;

DI f32x4 mfma32(u32x4 a, u32x4 b, f32x4 c) {
  return __builtin_amdgcn_mfma_f32_16x16x32_bf16(__builtin_bit_cast(bf16x8, a), __builtin_bit_cast(bf16x8, b), c, 0, 0, 0);
}
DI f32x4 mfma16(u32x2 a, u32x2 b, f32x4 c) {
  return __builtin_amdgcn_mfma_f32_16x16x16bf16_1k(__builtin_bit_cast(s16x4, a), __builtin_bit_cast(s16x4, b), c, 0, 0, 0);
}
DI u32 pk2(float a, float b) { f32x2 v = {a, b}; return __builtin_bit_cast(u32, __builtin_convertvector(v, bf16x2)); }
DI float lo16(u32 p) { return __uint_as_float(p << 16); }
DI float hi16(u32 p) { return __uint_as_float(p & 0xffff0000u); }
DI float bfr(float a) { return lo16(pk2(a, a)); }
DI u32x2 trd(u32 addr) {
  return __builtin_bit_cast(u32x2, __builtin_amdgcn_ds_read_tr16_b64_v4i16((LAS s16x4*)addr));
}
DI u32x4 cat(u32x2 a, u32x2 b) { return u32x4{a.x, a.y, b.x, b.y}; }
DI u32x4 ldsr128(u32 addr) { return *(LAS u32x4*)addr; }
DI void ldsw128(u32 addr, u32x4 v) { *(LAS u32x4*)addr = v; }
DI rsrc_t mkrs(const void* p, u32 bytes) { return __builtin_amdgcn_make_buffer_rsrc((void*)p, 0, bytes, 0x00020000); }
DI u32x4 ld128c(rsrc_t r, u32 vo, u32 so) { return __builtin_amdgcn_raw_buffer_load_b128(r, vo, so, 16); }
DI void st128c(rsrc_t r, u32 vo, u32x4 v) { __builtin_amdgcn_raw_buffer_store_b128(v, r, vo, 0, 16); }
DI void glds(rsrc_t r, u32 vo, u32 so, u32 lds) {
  asm volatile("s_mov_b32 m0, %3\n\ts_nop 0\n\tbuffer_load_dwordx4 %0, %1, %2 offen lds" ::"v"(vo), "s"(r), "s"(so), "s"(lds) : "memory");
}
#define VMCNT(n) asm volatile("s_waitcnt vmcnt(%0)" ::"i"(n) : "memory")
#define BARRIER() asm volatile("s_waitcnt lgkmcnt(0)\n\ts_barrier" ::: "memory")
template <int CTRL> DI float dppf(float v) { return __uint_as_float(__builtin_amdgcn_update_dpp(0u, __float_as_uint(v), CTRL, 0xf, 0xf, true)); }
DI float sum16(float v) { v += dppf<0xB1>(v); v += dppf<0x4E>(v); v += dppf<0x141>(v); v += dppf<0x140>(v); return v; }
DI float xsum(float v) {
  auto a = __builtin_amdgcn_permlane16_swap(__float_as_uint(v), __float_as_uint(v), false, false);
  v = __uint_as_float(a[0]) + __uint_as_float(a[1]);
  auto b = __builtin_amdgcn_permlane32_swap(__float_as_uint(v), __float_as_uint(v), false, false);
  return __uint_as_float(b[0]) + __uint_as_float(b[1]);
}
DI float xmax(float v) {
  auto a = __builtin_amdgcn_permlane16_swap(__float_as_uint(v), __float_as_uint(v), false, false);
  v = fmaxf(__uint_as_float(a[0]), __uint_as_float(a[1]));
  auto b = __builtin_amdgcn_permlane32_swap(__float_as_uint(v), __float_as_uint(v), false, false);
  return fmaxf(__uint_as_float(b[0]), __uint_as_float(b[1]));
}
DI float dot2(u32 a, u32 b, float c) { return __builtin_amdgcn_fdot2_f32_bf16(__builtin_bit_cast(bf16x2, a), __builtin_bit_cast(bf16x2, b), c, false); }
DI float sum32(float v) {
  v = sum16(v);
  auto a = __builtin_amdgcn_permlane16_swap(__float_as_uint(v), __float_as_uint(v), false, false);
  return __uint_as_float(a[0]) + __uint_as_float(a[1]);
}
#define SB() __builtin_amdgcn_sched_barrier(0)
DI u32 opq(u32 v) { asm volatile("" : "+s"(v)); return v; }
DI u32 opv(u32 v) { asm volatile("" : "+v"(v)); return v; }
#define PIN(x) asm volatile("" : "+v"(x))
template <int V> struct IC { static constexpr int v = V; };
template <int I, int N, class Fn> DI void sfor(Fn&& f) { if constexpr (I < N) { f(IC<I>{}); sfor<I + 1, N>(f); } }
DI u32 cw_ld(u32 addr) {
  u32 v; asm volatile("ds_read_b32 %0, %1\n\ts_waitcnt lgkmcnt(0)" : "=v"(v) : "v"(addr) : "memory");
  return __builtin_amdgcn_readfirstlane(v);
}
DI u32 cw_min4(u32 addr) {
  u32x4 v; asm volatile("ds_read_b128 %0, %1\n\ts_waitcnt lgkmcnt(0)" : "=v"(v) : "v"(addr) : "memory");
  return __builtin_amdgcn_readfirstlane(min(min(v.x, v.y), min(v.z, v.w)));
}
DI void cw_st(u32 addr, u32 val, int lane) {
  if (lane == 0) asm volatile("ds_write_b32 %0, %1" ::"v"(addr), "v"(val) : "memory");
}
DI void wait_min4(u32 addr, u32 need) {
#pragma unroll 1
  for (u32 sp = 0; sp < SPIN_L; ++sp) { if (cw_min4(addr) >= need) break; __builtin_amdgcn_s_sleep(1); }
}
DI void wait_word(u32 addr, u32 need) {
#pragma unroll 1
  for (u32 sp = 0; sp < SPIN_L; ++sp) { if (cw_ld(addr) >= need) break; __builtin_amdgcn_s_sleep(1); }
}
DI u32 cw_add(u32 addr, int lane) {
  u32 old = 0;
  if (lane == 0) asm volatile("ds_add_rtn_u32 %0, %1, %2\n\ts_waitcnt lgkmcnt(0)" : "=v"(old) : "v"(addr), "v"(1u) : "memory");
  return __builtin_amdgcn_readfirstlane(old);
}
DI void flag_set(char* ws, u32 base, int id, u64 tg, int lane) {
  if (lane < 8) __hip_atomic_store((GAS u64*)(ws + base + (lane * 256 + id) * 16), tg, __ATOMIC_RELAXED, __HIP_MEMORY_SCOPE_AGENT);
}
template <int N> DI void flag_wait(char* ws, u32 base, int rep, int id0, int id1, u64 tag) {
  GAS u64* p0 = (GAS u64*)(ws + base + (rep * 256 + id0) * 16);
  GAS u64* p1 = (GAS u64*)(ws + base + (rep * 256 + id1) * 16);
#pragma unroll 1
  for (u32 sp = 0; sp < SPIN_G; ++sp) {
    bool ok = __hip_atomic_load(p0, __ATOMIC_RELAXED, __HIP_MEMORY_SCOPE_AGENT) == tag;
    if (N > 1) ok &= __hip_atomic_load(p1, __ATOMIC_RELAXED, __HIP_MEMORY_SCOPE_AGENT) == tag;
    if (__all(ok)) break;
    __builtin_amdgcn_s_sleep(2);
  }
  __builtin_amdgcn_fence(__ATOMIC_ACQUIRE, "wavefront");
  asm volatile("" ::: "memory");
}
DI void publish(char* ws, u32 base, int id, u64 tag, u32 cnt, u32 nw, int lane) {
  VMCNT(0);
  if (cw_add(cnt, lane) == nw - 1) flag_set(ws, base, id, tag, lane);
}

#define NSTAMP 64
#define ST(i) do { u64 t_; asm volatile("s_memrealtime %0\n\ts_waitcnt lgkmcnt(0)" : "=s"(t_) :: "memory"); u32 t2_ = (u32)t_ | 1u; asm volatile("v_writelane_b32 %0, %1, %2" : "+v"(stampv) : "s"(t2_), "i"(i)); } while (0)

struct Ctx {
  const u16 *x, *wqkv, *wphi, *wo, *w1, *b1, *w2, *b2;
  u16* out;
  char* ws;
};

DI u32x4 hedgehog(f32x4 p) {
  const float L2E = 1.4426950408889634f;
  u32 a = pk2(p[0], p[1]), b = pk2(p[2], p[3]);
  float p0 = lo16(a) * L2E, p1 = hi16(a) * L2E, p2 = lo16(b) * L2E, p3 = hi16(b) * L2E;
  float mx = xmax(fmaxf(fmaxf(p0, p1), fmaxf(p2, p3)));
  float mn = -xmax(-fminf(fminf(p0, p1), fminf(p2, p3)));
  float e0 = __builtin_amdgcn_exp2f(p0 - mx), e1 = __builtin_amdgcn_exp2f(p1 - mx), e2 = __builtin_amdgcn_exp2f(p2 - mx), e3 = __builtin_amdgcn_exp2f(p3 - mx);
  float n0 = __builtin_amdgcn_exp2f(mn - p0), n1 = __builtin_amdgcn_exp2f(mn - p1), n2 = __builtin_amdgcn_exp2f(mn - p2), n3 = __builtin_amdgcn_exp2f(mn - p3);
  float rs = __builtin_amdgcn_rcpf(xsum((e0 + e1) + (e2 + e3)));
  float rn = __builtin_amdgcn_rcpf(xsum((n0 + n1) + (n2 + n3)));
  return u32x4{pk2(e0 * rs, e1 * rs), pk2(e2 * rs, e3 * rs), pk2(n0 * rn, n1 * rn), pk2(n2 * rn, n3 * rn)};
}

constexpr int c_need(int n) { return n < 9 ? (n == 0 ? 44 : n <= 2 ? 48 : n == 3 ? 52 : n <= 5 ? 56 : n == 6 ? 60 : 64) : 64 + 4 * (n - 8); }

__global__ __launch_bounds__(512, 2) void fused(const u16* __restrict__ x, const u16* __restrict__ wqkv, const u16* __restrict__ wphi,
                                                const u16* __restrict__ wo, const u16* __restrict__ w1, const u16* __restrict__ b1,
                                                const u16* __restrict__ w2, const u16* __restrict__ b2, u16* __restrict__ out, char* ws) {
#if __HIP_DEVICE_COMPILE__
  u32 stampv = 0;
  ST(0);
  const int tid = threadIdx.x, lane = tid & 63, w = __builtin_amdgcn_readfirstlane(tid >> 6);
  const int l15 = lane & 15, g = lane >> 4;
  const int bid = blockIdx.x, xc = bid & 7, r = bid >> 3;
  const bool isG = w < 4;
  const int hw = w & 3;
  const int stile = 8 * (xc >> 1) + (r & 7), dtile = 4 * (xc & 1) + (r >> 3);
  const bool roleA = r < 16;
  const int j = r & 15;
  const int b_dt = 4 * (xc & 1) + (j & 3), b_ft = 4 * (xc >> 1) + (j >> 2);
  const bool roleB = !roleA && b_ft < 15;
  const int a_rb = 4 * (xc >> 1) + (j & 3), a_grp = 4 * (xc & 1) + (j >> 2);
  const rsrc_t rws = mkrs(ws, WS_END);
  const rsrc_t rx = mkrs(x, S * D * 2);

  const u64 epoch = __hip_atomic_load((GAS u64*)(ws + WS_EPOCH), __ATOMIC_RELAXED, __HIP_MEMORY_SCOPE_AGENT);
#define tag (epoch + 1)
  if (tid < 64) *(LAS u32*)(L_CTL + tid * 4) = 0;
  BARRIER();
  ST(12);

  const u32 cswz = (((lane & 7) ^ (((lane >> 4) & 3) << 1)) << 4);
  const int crot = CROT;
  auto c_issue = [&](int n) {
    const u32 dst = ((6 + n) % 9) * 16384 + hw * 4096;
    if (n < 11) {
      const rsrc_t rw = mkrs(w2, F * S * 2);
      int ks = 4 * n + hw; if (ks > 42) ks = 42;
      ks += crot; if (ks >= 43) ks -= 43;
      const u32 vo = (lane >> 3) * 4096 + cswz + stile * 128;
#pragma unroll
      for (int m = 0; m < 4; ++m) glds(rw, vo, ks * 131072 + m * 32768, dst + m * 1024);
    } else {
      const rsrc_t rw = mkrs(wo, D * D * 2);
      const int kt = 4 * (n - 11) + hw;
      const u32 vo = (lane >> 3) * 1024 + cswz + dtile * 128;
#pragma unroll
      for (int m = 0; m < 4; ++m) glds(rw, vo, kt * 32768 + m * 8192, dst + m * 1024);
    }
  };
  int c_first = 0;
  u32 fullv = 0;
  auto pub = [&](u32 v) { if (v > fullv) { fullv = v; cw_st(L_FULL + hw * 4, v, lane); } };

  if (roleA) {
    const int rq = w & 3, hp = w >> 2;
    const rsrc_t rw = mkrs(wqkv, D * 3 * D * 2);
    const int row0 = a_rb * 128;
    const u32 xvo = (row0 + 16 * w + (lane >> 3)) * 1024, xsw = lane >> 4;
    const u32 wvo = (lane >> 3) * 3072 + (((lane & 7) ^ ((((lane >> 4) & 1) | ((w & 1) << 1)) << 1)) << 4) + a_grp * 128;
    const u32 xv0 = xvo + (((lane & 7) ^ xsw) << 4), xv1 = xvo + (((lane & 7) ^ (xsw + 4)) << 4);
    auto a_issue1 = [&](int c, int i) {
      const u32 dst = (c % 3) * 40960;
      if (i < 2) glds(rx, i ? xv1 : xv0, i * 8192 + c * 128, dst + w * 2048 + i * 1024);
      else glds(rw, wvo, (64 * c + 8 * w) * 3072 + (i - 2) * 1024, dst + 16384 + (i - 2) * 8192 + w * 1024);
    };
    u32 wpr[2][4];
#pragma unroll
    for (int e = 0; e < 2; ++e) {
      const u16* p = wphi + (a_grp * 4 + 2 * hp + e) * 256 + 4 * g * 16 + l15;
#pragma unroll
      for (int i = 0; i < 4; ++i) wpr[e][i] = p[16 * i];
    }
    SB();
#pragma unroll
    for (int i = 0; i < 10; ++i) a_issue1(i / 5, i % 5);
    ST(13);
    f32x4 aq[2][2], ak[2][2], av[2][2];
#pragma unroll
    for (int a = 0; a < 2; ++a)
#pragma unroll
      for (int e = 0; e < 2; ++e) { aq[a][e] = f32x4{0, 0, 0, 0}; ak[a][e] = aq[a][e]; av[a][e] = aq[a][e]; }
    const u32 xb0 = (32 * rq + l15) * 128 + ((g ^ ((l15 >> 1) & 7)) << 4);
    const u32 wf = ((l15 >> 3) & 1) | ((g & 1) << 1);
    u32 wb[2];
#pragma unroll
    for (int e = 0; e < 2; ++e) wb[e] = 16384 + (8 * g + (l15 >> 2)) * 128 + (((2 * hp + e) ^ wf) << 5) + (l15 & 3) * 8;
    u32x4 xa[2][2], wa[2][6];
    auto a_step = [&](bool mma, int kc, bool rd, int kr, u32 sb, int cn, int p0, int np) {
      u32 xb = 0, wbb[2] = {0, 0};
      if (rd) { xb = opv(sb + (xb0 ^ (kr * 64))); wbb[0] = opv(sb + wb[0]); wbb[1] = opv(sb + wb[1]); }
#pragma unroll
      for (int q = 0; q < 6; ++q) {
        const int e = q / 3, sec = q % 3;
        if (rd) {
          if (q < 2) xa[kr][q] = ldsr128(xb + q * 2048);
          const u32 ad = wbb[e] + sec * 8192 + kr * 4096;
          wa[kr][q] = cat(trd(ad), trd(ad + 512));
        }
        SB();
        if (mma) {
#pragma unroll
          for (int a = 0; a < 2; ++a) {
            if (sec == 0) { aq[a][e] = mfma32(wa[kc][q], xa[kc][a], aq[a][e]); PIN(aq[a][e]); }
            if (sec == 1) { ak[a][e] = mfma32(wa[kc][q], xa[kc][a], ak[a][e]); PIN(ak[a][e]); }
            if (sec == 2) { av[a][e] = mfma32(xa[kc][a], wa[kc][q], av[a][e]); PIN(av[a][e]); }
          }
        }
        if ((q & 1) && q / 2 < np) a_issue1(cn, p0 + q / 2);
        SB();
      }
    };
    u64 early = 0;
    GAS u64* const pfl = (GAS u64*)(ws + (lane < 8 ? WS_FH + (xc * 256 + a_rb * 8 + lane) * 16 : WS_FCS + (xc * 256 + a_grp) * 16));
    u32x2 stl[2]; u32x4 stv[2][2], csq[2], csk[2]; u32 csv[2];
#pragma unroll
    for (int c = 0; c < 8; ++c) {
      if (c == 7 && w == 0) {
        VMCNT(0);
        asm volatile("" : "+v"(early));
        if (!__all(early == tag)) flag_wait<1>(ws, lane < 8 ? WS_FH : WS_FCS, xc, lane < 8 ? a_rb * 8 + lane : a_grp, 0, tag);
      }
      if (c < 7) VMCNT(5); else VMCNT(0);
      BARRIER();
      ST(16 + c);
      const u32 sb = (c % 3) * 40960;
      if (c == 7) {
#pragma unroll
        for (int a = 0; a < 2; ++a) {
          const u32 rb8 = WS_STAT + (row0 + 32 * rq + 16 * a) * 8;
          stl[a] = __builtin_amdgcn_raw_buffer_load_b64(rws, rb8 + l15 * 8, 0, 16);
          stv[a][0] = ld128c(rws, rb8 + g * 32, 0);
          stv[a][1] = ld128c(rws, rb8 + g * 32 + 16, 0);
        }
#pragma unroll
        for (int e = 0; e < 2; ++e) {
          const u32 cb = WS_CS + (a_grp * 64 + (2 * hp + e) * 16) * 4;
          csq[e] = ld128c(rws, cb + g * 16, 0);
          csk[e] = ld128c(rws, cb + 2048 + g * 16, 0);
          csv[e] = __builtin_amdgcn_raw_buffer_load_b32(rws, cb + 4096 + l15 * 4, 0, 16);
        }
      }
      if (c == 6 && w == 0) asm volatile("global_load_dwordx2 %0, %1, off sc1" : "=v"(early) : "v"(pfl) : "memory");
      a_step(c > 0, 1, true, 0, sb, c + 2, 0, c < 6 ? 2 : 0);
      a_step(true, 0, true, 1, sb, c + 2, 2, c < 6 ? 3 : 0);
    }
    a_step(true, 1, false, 0, 0, 0, 0, 0);
    u32x2 wph[2];
#pragma unroll
    for (int e = 0; e < 2; ++e) wph[e] = u32x2{wpr[e][0] | (wpr[e][1] << 16), wpr[e][2] | (wpr[e][3] << 16)};
    ST(2);
    BARRIER();
    const u32 one2 = 0x3f803f80u;
    u32x4 selP = {0, 0, 0, 0}, selN = {0, 0, 0, 0};
    {
      const int dlt = l15 - 4 * g;
      const u32 v0 = dlt == 0 ? 0x3f80u : dlt == 1 ? 0x3f800000u : 0u, v1 = dlt == 2 ? 0x3f80u : dlt == 3 ? 0x3f800000u : 0u;
      selP.x = v0; selP.y = v1; selN.z = v0; selN.w = v1;
    }
    auto fold = [&](f32x4 acc, float m, float ri, u32x4 cs) {
      return u32x2{pk2((acc[0] - m * __uint_as_float(cs.x)) * ri, (acc[1] - m * __uint_as_float(cs.y)) * ri),
                   pk2((acc[2] - m * __uint_as_float(cs.z)) * ri, (acc[3] - m * __uint_as_float(cs.w)) * ri)};
    };
    const f32x4 zero4 = {0, 0, 0, 0};
#pragma unroll
    for (int e = 0; e < 2; ++e) {
      u32x2 tp[2], tn[2], vb[2];
#pragma unroll
      for (int a = 0; a < 2; ++a) {
        const u32x4 ph = hedgehog(mfma16(wph[e], fold(ak[a][e], __uint_as_float(stl[a].x), __uint_as_float(stl[a].y), csk[e]), zero4));
        const f32x4 p4 = mfma32(ph, selP, zero4), n4 = mfma32(ph, selN, zero4);
        tp[a] = u32x2{pk2(p4[0], p4[1]), pk2(p4[2], p4[3])};
        tn[a] = u32x2{pk2(n4[0], n4[1]), pk2(n4[2], n4[3])};
        const float cv = __uint_as_float(csv[e]);
        const f32x4 t = av[a][e];
        vb[a] = u32x2{pk2((t[0] - __uint_as_float(stv[a][0].x) * cv) * __uint_as_float(stv[a][0].y), (t[1] - __uint_as_float(stv[a][0].z) * cv) * __uint_as_float(stv[a][0].w)),
                      pk2((t[2] - __uint_as_float(stv[a][1].x) * cv) * __uint_as_float(stv[a][1].y), (t[3] - __uint_as_float(stv[a][1].z) * cv) * __uint_as_float(stv[a][1].w))};
      }
      const u32x4 P = cat(tp[0], tp[1]), N = cat(tn[0], tn[1]), V = cat(vb[0], vb[1]), ones = {one2, one2, one2, one2};
      const f32x4 kvp = mfma32(P, V, zero4), kvn = mfma32(N, V, zero4), zp = mfma32(P, ones, zero4), zn = mfma32(N, ones, zero4);
      const int ti = (2 * hp + e) * 2;
      ldsw128(L_AKV + (ti * 4 + rq) * 1024 + lane * 16, __builtin_bit_cast(u32x4, kvp));
      ldsw128(L_AKV + ((ti + 1) * 4 + rq) * 1024 + lane * 16, __builtin_bit_cast(u32x4, kvn));
      if (l15 == 0) {
        ldsw128(L_AZ + (ti * 4 + rq) * 64 + g * 16, __builtin_bit_cast(u32x4, zp));
        ldsw128(L_AZ + ((ti + 1) * 4 + rq) * 64 + g * 16, __builtin_bit_cast(u32x4, zn));
      }
    }
    BARRIER();
    ST(3);
    if (isG) {
      f32x4 sp = zero4, sn = zero4;
      float zz = 0.f;
#pragma unroll
      for (int q = 0; q < 4; ++q) {
        sp += __builtin_bit_cast(f32x4, ldsr128(L_AKV + (w * 8 + q) * 1024 + lane * 16));
        sn += __builtin_bit_cast(f32x4, ldsr128(L_AKV + (w * 8 + 4 + q) * 1024 + lane * 16));
        zz += *(LAS float*)(L_AZ + (w * 8 + ((lane >> 4) & 1) * 4 + q) * 64 + l15 * 4);
      }
      const u32 rec = a_rb * 32 + a_grp * 4 + w;
      st128c(rws, WS_KV + rec * 1024 + lane * 16, u32x4{pk2(sp[0], sp[1]), pk2(sp[2], sp[3]), pk2(sn[0], sn[1]), pk2(sn[2], sn[3])});
      if (lane < 32) __builtin_amdgcn_raw_buffer_store_b32(__float_as_uint(zz), rws, WS_Z + rec * 128 + lane * 4, 0, 16);
      publish(ws, WS_FKV, a_rb * 8 + a_grp, tag, L_CNT, 4, lane);
    } else {
#pragma unroll
      for (int n = 0; n < 6; ++n) c_issue(n);
      c_first = 6;
    }
    ST(4);
    u32x4 phq[2][2];
#pragma unroll
    for (int a = 0; a < 2; ++a)
#pragma unroll
      for (int e = 0; e < 2; ++e)
        phq[a][e] = hedgehog(mfma16(wph[e], fold(aq[a][e], __uint_as_float(stl[a].x), __uint_as_float(stl[a].y), csq[e]), zero4));
    ST(5);
    if (isG) {
      if (w == 0) { flag_wait<1>(ws, WS_FKV, xc, (lane & 15) * 8 + a_grp, 0, tag); cw_st(L_LND, 1, lane); }
      else wait_word(L_LND, 1);
      ST(6);
      u32x4 kvl[16]; u32 zl[16];
#pragma unroll
      for (int q = 0; q < 16; ++q) {
        const u32 rec = q * 32 + a_grp * 4 + w;
        kvl[q] = ld128c(rws, WS_KV + lane * 16, rec * 1024);
        zl[q] = __builtin_amdgcn_raw_buffer_load_b32(rws, WS_Z + (lane & 31) * 4, rec * 128, 16);
      }
      SB();
      const u32 selo = opv(0x3f80u), sehi = opv(0x3f800000u);
      float a8[8], zz = 0.f;
#pragma unroll
      for (int i = 0; i < 8; ++i) a8[i] = 0.f;
#pragma unroll
      for (int q = 0; q < 16; ++q) {
#pragma unroll
        for (int i = 0; i < 4; ++i) { a8[2 * i] = dot2(kvl[q][i], selo, a8[2 * i]); a8[2 * i + 1] = dot2(kvl[q][i], sehi, a8[2 * i + 1]); }
        zz += __uint_as_float(zl[q]);
      }
      ldsw128(L_AT + w * 1024 + lane * 16, u32x4{pk2(a8[0], a8[1]), pk2(a8[2], a8[3]), pk2(a8[4], a8[5]), pk2(a8[6], a8[7])});
      if (lane < 32) *(LAS u16*)(L_AZT + w * 64 + lane * 2) = (u16)pk2(zz, zz);
    }
    BARRIER();
    ST(14);
    {
      u32x4 akv[2], az[2];
#pragma unroll
      for (int e = 0; e < 2; ++e) {
        akv[e] = ldsr128(L_AT + (2 * hp + e) * 1024 + lane * 16);
        az[e] = cat(*(LAS u32x2*)(L_AZT + (2 * hp + e) * 64 + g * 8), *(LAS u32x2*)(L_AZT + (2 * hp + e) * 64 + 32 + g * 8));
      }
#pragma unroll
      for (int a = 0; a < 2; ++a) {
        u32x2 ov[2];
#pragma unroll
        for (int e = 0; e < 2; ++e) {
          const f32x4 num = mfma32(akv[e], phq[a][e], zero4), den = mfma32(az[e], phq[a][e], zero4);
          const float rd = __builtin_amdgcn_rcpf(bfr(bfr(den[0]) + 1e-6f));
          const u32 n01 = pk2(num[0], num[1]), n23 = pk2(num[2], num[3]);
          ov[e] = u32x2{pk2(lo16(n01) * rd, hi16(n01) * rd), pk2(lo16(n23) * rd, hi16(n23) * rd)};
        }
        st128c(rws, WS_O + ((a_grp * 2 + hp) * 128 + a_rb * 8 + 2 * rq + a) * 1024 + lane * 16, cat(ov[0], ov[1]));
      }
      publish(ws, WS_FO, a_rb * 8 + a_grp, tag, L_CNT + 4, 8, lane);
      if (!isG) pub(16 + 6);
    }
    BARRIER();
    if (isG) cw_st(L_PROG + hw * 4, 64, lane);
    ST(7);
  } else {
    const int li = xc * 16 + j;
    const int rot = BROT;
    const rsrc_t rw1 = mkrs(w1, S * F * 2);
    u32 vo[6];
    u32x4 cst[24];
    auto b_issue = [&](int m) {
      const u32 so = ((4 * m + hw + rot) & 63) * (32 * F * 2);
#pragma unroll
      for (int n = 0; n < 6; ++n) glds(rw1, vo[n], so, (m % 6) * 24576 + hw * 6144 + n * 1024);
    };
    {
      const int rl = 2 * w + (lane >> 5), row = li * 16 + rl;
      u32x4 xr[2];
      asm volatile("buffer_load_dwordx4 %0, %2, %3, 0 offen\n\tbuffer_load_dwordx4 %1, %2, %3, 0 offen offset:512"
                   : "=&v"(xr[0]), "=&v"(xr[1]) : "v"(row * 1024 + (lane & 31) * 16), "s"(rx) : "memory");
      if (!isG && roleB) {
#pragma unroll
        for (int n = 0; n < 6; ++n) {
          const int c = n * 64 + lane, rr = c / 12;
          int ch = (c % 12) ^ (((rr >> 3) & 1) << 1);
          if (b_ft == 14) ch &= 3;
          vo[n] = rr * (F * 2) + ch * 16 + b_ft * 192;
        }
        b_issue(0);
        ST(13);
        VMCNT(6);
      } else if (!roleB) {
        const rsrc_t rw = mkrs(wqkv, D * 3 * D * 2);
#pragma unroll
        for (int i = 0; i < 24; ++i)
          cst[i] = __builtin_amdgcn_raw_buffer_load_b128(rw, (w * 64 + (lane >> 3)) * 3072 + ((xc & 1) * 4 + (j & 3)) * 128 + (lane & 7) * 16, (i / 3) * 8 * 3072 + (i % 3) * 1024, 0);
        SB();
        VMCNT(24);
      } else {
        ST(13);
        VMCNT(0);
      }
      asm volatile("" : "+v"(xr[0]), "+v"(xr[1]));
      ST(1);
      u32 pk[8] = {xr[0].x, xr[0].y, xr[0].z, xr[0].w, xr[1].x, xr[1].y, xr[1].z, xr[1].w};
#pragma unroll
      for (int pass = 0; pass < 2; ++pass) {
        float s1 = 0.f, s2 = 0.f;
#pragma unroll
        for (int i = 0; i < 8; ++i) { s1 = dot2(pk[i], 0x3f803f80u, s1); s2 = dot2(pk[i], pk[i], s2); }
        const float m = sum32(s1) * (1.f / D);
        const float var = bfr(fmaxf(sum32(s2) * (1.f / D) - m * m, 0.f));
        const float mb = bfr(m);
        const float ri = __builtin_amdgcn_rcpf(bfr(__builtin_amdgcn_sqrtf(bfr(var + 1e-5f))));
#pragma unroll
        for (int i = 0; i < 8; ++i) pk[i] = pk2((lo16(pk[i]) - mb) * ri, (hi16(pk[i]) - mb) * ri);
        if (pass == 0 && (lane & 31) == 0) *(LAS u32x2*)(L_LNS + rl * 8) = u32x2{__float_as_uint(mb), __float_as_uint(ri)};
      }
      const u32 wa = L_LN + rl * LN_PITCH + (rl >> 3) * 32 + (lane & 31) * 16;
      ldsw128(wa, u32x4{pk[0], pk[1], pk[2], pk[3]});
      ldsw128(wa + 512, u32x4{pk[4], pk[5], pk[6], pk[7]});
      cw_add(L_CNT + 8, lane);
      ST(14);
    }
    if (isG) {
      wait_word(L_CNT + 8, 8);
      ST(15);
#pragma unroll
      for (int rr = 0; rr < 4; ++rr) {
        const int item = w * 16 + rr * 4 + g, dt = item >> 1, oct = item & 1;
        const u32 ad = L_LN + (8 * oct + (l15 >> 2)) * LN_PITCH + oct * 32 + dt * 32 + (l15 & 3) * 8;
        const u32x4 hv = cat(trd(ad), trd(ad + 4 * LN_PITCH));
        st128c(rws, WS_H + (((li >> 1) * 32 + dt) * 4 + (li & 1) * 2 + oct) * 256 + l15 * 16, hv);
      }
      if (w == 0 && lane < 8) st128c(rws, WS_STAT + li * 128 + lane * 16, ldsr128(L_LNS + lane * 16));
      ST(2);
      VMCNT(0);
      if (cw_add(L_CNT + 12, lane) == 3) { flag_set(ws, WS_FH, li, tag, lane); cw_st(L_LND, 1, lane); }
      ST(3);
    }
    if (!roleB) {
      const int grp = (xc & 1) * 4 + (j & 3);
      const u32 sv = (l15 & 1) ? 0x3f800000u : 0x3f80u;
      const u32x4 sel = {l15 >> 1 == 0 ? sv : 0u, l15 >> 1 == 1 ? sv : 0u, l15 >> 1 == 2 ? sv : 0u, l15 >> 1 == 3 ? sv : 0u};
      f32x4 cs[3] = {{0, 0, 0, 0}, {0, 0, 0, 0}, {0, 0, 0, 0}};
#pragma unroll
      for (int i = 0; i < 24; ++i) cs[i % 3] = mfma32(cst[i], sel, cs[i % 3]);
#pragma unroll
      for (int sec = 0; sec < 3; ++sec) ldsw128((w * 3 + sec) * 1024 + lane * 16, __builtin_bit_cast(u32x4, cs[sec]));
      if (cw_add(L_CNT, lane) == 7) {
        const int pos = lane >> 3, jj = lane & 7;
        float o3[3];
#pragma unroll
        for (int sec = 0; sec < 3; ++sec) {
          float a = 0.f;
#pragma unroll
          for (int q = 0; q < 8; ++q) {
            const u32 ad = (q * 3 + sec) * 1024 + ((pos >> 2) * 16 + jj) * 16 + (pos & 3) * 4;
            a += *(LAS float*)ad + *(LAS float*)(ad + 512);
          }
          o3[sec] = a;
        }
#pragma unroll
        for (int sec = 0; sec < 3; ++sec) __builtin_amdgcn_raw_buffer_store_b32(__float_as_uint(o3[sec]), rws, WS_CS + sec * 2048 + grp * 256 + lane * 4, 0, 16);
        VMCNT(0);
        flag_set(ws, WS_FCS, grp, tag, lane);
        cw_st(L_CNT + 4, 1, lane);
        ST(4);
      }
    }
    if (isG) {
    if (roleB) {
      u32x2 bias[6];
#pragma unroll
      for (int t = 0; t < 6; ++t) { int f = b_ft * 96 + t * 16 + 4 * g; if (f > F - 4) f = F - 4; bias[t] = *(const GAS u32x2*)(b1 + f); }
      if (w == 0) {
        flag_wait<2>(ws, WS_FH, xc, lane, lane + 64, tag);
        cw_st(L_GO, 1, lane);
      } else {
        wait_word(L_GO, 1);
      }
      ST(4);
      const u32 hvo = (b_dt * 4 + w) * 1024 + lane * 16;
      constexpr int P = 16;
      u32x4 hr[P];
#pragma unroll
      for (int i = 0; i < P; ++i) hr[i] = ld128c(rws, hvo, WS_H + ((i + rot) & 63) * 32768);
      __builtin_amdgcn_sched_barrier(0);
      f32x4 acc[6];
#pragma unroll
      for (int t = 0; t < 6; ++t) acc[t] = f32x4{0, 0, 0, 0};
      const u32 rowb = (8 * g + (l15 >> 2)) * 192 + (l15 & 3) * 8;
      const u32 bE = rowb + (g & 1) * 32, bO = rowb - (g & 1) * 32;
      u32x4 wfr[2][6];
      u32 avail = 0;
      auto b_need = [&](u32 need) { if (avail < need) { wait_min4(L_FULL, need); avail = cw_min4(L_FULL); } };
      u32 gE = bE, gO = bO;
      auto b_rd = [&](int buf, int k, int t) {
        const u32 ad = ((t & 1) ? gO : gE) + k * 6144 + t * 32;
        wfr[buf][t] = cat(trd(ad), trd(ad + 768));
      };
      b_need(1);
#pragma unroll
      for (int t = 0; t < 6; ++t) b_rd(0, 0, t);
      u32 pos = 0;
#pragma unroll 1
      for (int mo = 0; mo < 4; ++mo) {
        const u32 hv2 = mo < 3 ? hvo : 0x80000000u;
#pragma unroll
        for (int mi = 0; mi < 4; ++mi) {
          const int m = mo * 4 + mi;
          ST(16 + mi);
#pragma unroll
          for (int k = 0; k < 4; ++k) {
            if (k == 3) {
              cw_st(L_PROG + w * 4, 4 * (m + 1), lane);
              pos = pos == 5 ? 0 : pos + 1; gE = opv(bE + pos * 24576); gO = opv(bO + pos * 24576);
              b_need(m < 15 ? m + 2 : 0);
            }
#pragma unroll
            for (int t = 0; t < 6; ++t) {
              b_rd((k + 1) & 1, (k + 1) & 3, t);
              SB();
              acc[t] = mfma32(wfr[k & 1][t], hr[mi * 4 + k], acc[t]);
              PIN(acc[t]);
              SB();
            }
            hr[mi * 4 + k] = ld128c(rws, hv2, WS_H + ((m * 4 + k + P + rot) & 63) * 32768);
            SB();
          }
        }
      }
      ST(5);
#pragma unroll
      for (int m3 = 0; m3 < 3; ++m3) {
        u32x4 uv;
#pragma unroll
        for (int h2 = 0; h2 < 2; ++h2) {
          const int t = 2 * m3 + h2;
          float o4[4];
#pragma unroll
          for (int i = 0; i < 4; ++i) {
            const u32 bw = i < 2 ? bias[t].x : bias[t].y;
            const float v = acc[t][i] + ((i & 1) ? hi16(bw) : lo16(bw));
            const float zz = v * (1.f + 0.044715f * v * v) * (-2.f * 0.7978845608028654f * 1.4426950408889634f);
            o4[i] = v * __builtin_amdgcn_rcpf(1.f + __builtin_amdgcn_exp2f(zz));
          }
          uv[2 * h2] = pk2(o4[0], o4[1]); uv[2 * h2 + 1] = pk2(o4[2], o4[3]);
        }
        if (b_ft * 3 + m3 < 43) st128c(rws, WS_U + ((b_ft * 3 + m3) * 32 + b_dt * 4 + w) * 1024 + lane * 16, uv);
      }
      ST(6);
      publish(ws, WS_FU, b_dt * 16 + b_ft, tag, L_CNT, 4, lane);
      ST(7);
    } else {
      wait_word(L_LND, 1);
      cw_st(L_PROG + w * 4, 64, lane);
    }
    } else if (roleB) {
#pragma unroll
    for (int m = 1; m < 16; ++m) {
      if (m == 5) wait_word(L_LND, 1);
      if (m >= 6) {
        if (cw_min4(L_PROG) < 4 * (m - 5)) { VMCNT(0); pub(m); wait_min4(L_PROG, 4 * (m - 5)); }
      }
      b_issue(m);
      if (m >= 3) { VMCNT(18); pub(m - 2); }
      ST(16 + m);
    }
    VMCNT(12); pub(14); VMCNT(6); pub(15); VMCNT(0); pub(16);
    ST(4);
  } else {
    wait_word(L_CNT + 4, 1);
    }
  }

  if (isG) {
    const int srow = stile * 64 + l15;
    u32x2 xin[4]; u32 b2v[4];
#pragma unroll
    for (int t = 0; t < 4; ++t) {
      xin[t] = *(const GAS u32x2*)(x + (srow + 16 * t) * D + dtile * 64 + w * 16 + 4 * g);
      b2v[t] = b2[srow + 16 * t];
    }
    if (w == 0) {
      flag_wait<1>(ws, WS_FU, xc, dtile * 16 + (l15 < 15 ? l15 : 0), 0, tag);
      cw_st(L_GO, 2, lane);
    } else {
      wait_word(L_GO, 2);
    }
    ST(8);
    constexpr int P = 16;
    const u32 uvo = (dtile * 4 + w) * 1024 + lane * 16, ovo = (stile * 4 + w) * 1024 + lane * 16;
    auto uoff = [&](int i) { int ks = i + crot; if (ks >= 43) ks -= 43; return WS_U + ks * 32768; };
    u32x4 ar[P];
#pragma unroll
    for (int i = 0; i < P; ++i) ar[i] = ld128c(rws, uvo, uoff(i));
    __builtin_amdgcn_sched_barrier(0);
    f32x4 ay[4], as[4];
#pragma unroll
    for (int t = 0; t < 4; ++t) { ay[t] = f32x4{0, 0, 0, 0}; as[t] = ay[t]; }
    u32 tb[4];
    {
      const int row = 4 * g + (l15 >> 2), f = (row >> 1) & 3;
#pragma unroll
      for (int t = 0; t < 4; ++t) tb[t] = row * 128 + ((t ^ f) << 5) + (l15 & 3) * 8;
    }
    u32x4 wfr[3][4];
    u32 avail = 0;
    auto c_needf = [&](u32 need) { if (avail < need) { wait_min4(L_FULL, need); avail = cw_min4(L_FULL); } };
    u32 tg[4];
    auto c_pre = [&](auto ic) {
      constexpr int i = decltype(ic)::v;
      if (i < 60 && (i & 3) == 0) {
        c_needf(16 + i / 4 + 1);
#pragma unroll
        for (int t = 0; t < 4; ++t) tg[t] = opv(tb[t] + ((6 + i / 4) % 9) * 16384);
      }
    };
    auto c_rd = [&](auto ic, int t) {
      constexpr int i = decltype(ic)::v;
      if (i == 43 || i >= 60) return;
      const u32 ad = tg[t] + (i & 3) * 4096;
      wfr[i % 3][t] = cat(trd(ad), trd(ad + 2048));
    };
    c_pre(IC<0>{});
#pragma unroll
    for (int t = 0; t < 4; ++t) { c_rd(IC<0>{}, t); }
#pragma unroll
    for (int t = 0; t < 4; ++t) { c_rd(IC<1>{}, t); }
    sfor<0, 60>([&](auto ic) {
      constexpr int i = decltype(ic)::v;
      if ((i & 3) == 0) ST(32 + i / 4);
      if (i == 36) {
        wait_word(L_GOO, 1);
        ST(9);
#pragma unroll
        for (int q = 44; q < 52; ++q) ar[q % P] = ld128c(rws, ovo, WS_O + (q - 44) * 131072);
      }
      c_pre(IC<i + 2>{});
#pragma unroll
      for (int t = 0; t < 4; ++t) {
        c_rd(IC<i + 2>{}, t);
        SB();
        if (i < 43) { ay[t] = mfma32(ar[i % P], wfr[i % 3][t], ay[t]); PIN(ay[t]); }
        if (i > 43) { as[t] = mfma32(wfr[i % 3][t], ar[i % P], as[t]); PIN(as[t]); }
        SB();
      }
      if (((i + 2) & 3) == 3 && i + 2 < 60) cw_st(L_PROG + w * 4, 64 + 4 * ((i + 2) / 4 + 1), lane);
      constexpr int nx = i + P;
      if (nx < 43) ar[i % P] = ld128c(rws, uvo, uoff(nx));
      else if (nx >= 52 && nx < 60) ar[i % P] = ld128c(rws, ovo, WS_O + (nx - 44) * 131072);
      SB();
    });
    ST(10);
#pragma unroll
    for (int t = 0; t < 4; ++t)
      if (t != w) ldsw128(L_SCR + (t * 4 + w) * 1024 - (w > t ? 1024 : 0) - t * 1024 + lane * 16, __builtin_bit_cast(u32x4, as[t]));
    BARRIER();
#pragma unroll
    for (int t = 0; t < 4; ++t) {
      f32x4 sv;
      if (t == w) sv = w == 0 ? as[0] : w == 1 ? as[1] : w == 2 ? as[2] : as[3];
      else sv = __builtin_bit_cast(f32x4, ldsr128(L_SCR + (w * 3 + t - (t > w ? 1 : 0)) * 1024 + lane * 16));
      const float bb = lo16(b2v[t]);
      float o4[4];
#pragma unroll
      for (int i = 0; i < 4; ++i) {
        const float yy = ay[t][i] + bb;
        const float gg = yy * __builtin_amdgcn_rcpf(1.f + __builtin_amdgcn_exp2f(yy * -1.4426950408889634f));
        const u32 xw = i < 2 ? xin[t].x : xin[t].y;
        o4[i] = ((i & 1) ? hi16(xw) : lo16(xw)) + sv[i] * gg;
      }
      *(GAS u32x2*)(out + (srow + 16 * t) * D + dtile * 64 + w * 16 + 4 * g) = u32x2{pk2(o4[0], o4[1]), pk2(o4[2], o4[3])};
    }
  } else {
#pragma unroll
    for (int n = 0; n < 15; ++n) {
      if (n >= c_first) {
        if (cw_min4(L_PROG) < c_need(n)) { VMCNT(0); pub(16 + n); wait_min4(L_PROG, c_need(n)); }
        c_issue(n);
        VMCNT(12);
        pub(16 + n - 2);
        ST(32 + n);
      }
    }
    VMCNT(0);
    pub(31);
    ST(8);
    if (w == 4) {
      flag_wait<1>(ws, WS_FO, xc, (stile >> 1) * 8 + (lane & 7), 0, tag);
      cw_st(L_GOO, 1, lane);
      if (bid == 0 && lane == 0) __hip_atomic_store((GAS u64*)(ws + WS_EPOCH), tag, __ATOMIC_RELAXED, __HIP_MEMORY_SCOPE_AGENT);
    }
    ST(9);
    BARRIER();
  }

  if (bid == AMP_BID && w == AMP_W) {
    u32 dlt;
    dlt = 0;
    if (lane == 0) __hip_atomic_store((GAS u32*)(ws + WS_CS + 7168 + 64), (u32)__builtin_amdgcn_s_memrealtime(), __ATOMIC_RELAXED, __HIP_MEMORY_SCOPE_AGENT);
    if (dlt > 5000) dlt = 5000;
    u64 t0 = __builtin_amdgcn_s_memrealtime();
    while (__builtin_amdgcn_s_memrealtime() - t0 < (u64)dlt * 20) {}
  }
#endif
}

__global__ void tiny_pre(char* ws) { if (threadIdx.x == 0 && blockIdx.x == 0) __hip_atomic_store((GAS u32*)(ws + WS_CS + 7168), (u32)__builtin_amdgcn_s_memrealtime(), __ATOMIC_RELAXED, __HIP_MEMORY_SCOPE_AGENT); }
__global__ void tiny_post(char* ws) {
  u32 now = (u32)__builtin_amdgcn_s_memrealtime();
  u32 e = __hip_atomic_load((GAS u32*)(ws + WS_CS + 7168 + 64), __ATOMIC_RELAXED, __HIP_MEMORY_SCOPE_AGENT);
  u32 dlt = now - e; if (dlt > 5000) dlt = 5000;
}
#undef tag
}

extern "C" void kernel_launch(void* const* d_in, const int* in_sizes, int n_in, void* d_out, int out_size, void* d_ws, size_t ws_size,
                              hipStream_t stream) {
  using namespace hk;
  (void)hipFuncSetAttribute((const void*)fused, hipFuncAttributeMaxDynamicSharedMemorySize, LDS_BYTES);
  tiny_pre<<<1, 64, 0, stream>>>((char*)d_ws);
  fused<<<256, 512, LDS_BYTES, stream>>>((const u16*)d_in[0], (const u16*)d_in[1], (const u16*)d_in[2], (const u16*)d_in[3], (const u16*)d_in[5],
                                         (const u16*)d_in[6], (const u16*)d_in[7], (const u16*)d_in[8], (u16*)d_out, (char*)d_ws);
  tiny_post<<<1, 64, 0, stream>>>((char*)d_ws);
}
```

```cpp
#define STAMPS
#define AMP 2
#define AMP_BID 128
#define AMP_W 0
#define AMP_A 2
#define AMP_B 3
#include <hip/hip_runtime.h>
#include <stdint.h>

namespace hk {
typedef unsigned short u16;
typedef unsigned u32;
typedef unsigned long long u64;
typedef u32 u32x4 __attribute__((ext_vector_type(4)));
typedef u32 u32x2 __attribute__((ext_vector_type(2)));
typedef float f32x4 __attribute__((ext_vector_type(4)));
typedef float f32x2 __attribute__((ext_vector_type(2)));
typedef __bf16 bf16x8 __attribute__((ext_vector_type(8)));
typedef __bf16 bf16x2 __attribute__((ext_vector_type(2)));
typedef short s16x4 __attribute__((ext_vector_type(4)));
#define LAS __attribute__((address_space(3)))
#define GAS __attribute__((address_space(1)))
#define DI __device__ __forceinline__
typedef __amdgpu_buffer_rsrc_t rsrc_t;

#define CROT 0
#define BROT 0
constexpr int S = 2048, D = 512, F = 1376;
constexpr u32 WS_EPOCH = 0;
constexpr u32 FL_SZ = 8 * 256 * 16;
constexpr u32 WS_FH = 4096, WS_FKV = WS_FH + FL_SZ, WS_FO = WS_FKV + FL_SZ, WS_FU = WS_FO + FL_SZ, WS_FCS = WS_FU + FL_SZ;
constexpr u32 WS_STAT = WS_FCS + FL_SZ;
constexpr u32 WS_CS = WS_STAT + S * 8;
constexpr u32 WS_Z = WS_CS + 8192;
constexpr u32 WS_KV = WS_Z + 16 * 32 * 128;
constexpr u32 WS_H = WS_KV + 16 * 32 * 1024;
constexpr u32 WS_O = WS_H + S * D * 2;
constexpr u32 WS_U = WS_O + S * D * 2;
constexpr u32 WS_END = WS_U + D * F * 2;
constexpr u32 WS_STAMP = 64u << 20;
constexpr u32 RING = 147456, L_SCR = RING, L_CTL = 163840 - 256, LDS_BYTES = 163840;
constexpr u32 L_FULL = L_CTL, L_PROG = L_CTL + 16, L_GO = L_CTL + 32, L_GOO = L_CTL + 36, L_LND = L_CTL + 40, L_CNT = L_CTL + 48;
constexpr u32 L_LN = RING - 2048, LN_PITCH = 1088, L_LNS = L_LN + 16 * LN_PITCH + 64;
constexpr u32 L_AKV = 49152, L_AZ = L_AKV + 32768, L_AT = L_AZ + 2048, L_AZT = L_AT + 4096;
constexpr u32 SPIN_G = 1u << 15, SPIN_L = 1u << 20;

DI f32x4 mfma32(u32x4 a, u32x4 b, f32x4 c) {
  return __builtin_amdgcn_mfma_f32_16x16x32_bf16(__builtin_bit_cast(bf16x8, a), __builtin_bit_cast(bf16x8, b), c, 0, 0, 0);
}
DI f32x4 mfma16(u32x2 a, u32x2 b, f32x4 c) {
  return __builtin_amdgcn_mfma_f32_16x16x16bf16_1k(__builtin_bit_cast(s16x4, a), __builtin_bit_cast(s16x4, b), c, 0, 0, 0);
}
DI u32 pk2(float a, float b) { f32x2 v = {a, b}; return __builtin_bit_cast(u32, __builtin_convertvector(v, bf16x2)); }
DI float lo16(u32 p) { return __uint_as_float(p << 16); }
DI float hi16(u32 p) { return __uint_as_float(p & 0xffff0000u); }
DI float bfr(float a) { return lo16(pk2(a, a)); }
DI u32x2 trd(u32 addr) {
  return __builtin_bit_cast(u32x2, __builtin_amdgcn_ds_read_tr16_b64_v4i16((LAS s16x4*)addr));
}
DI u32x4 cat(u32x2 a, u32x2 b) { return u32x4{a.x, a.y, b.x, b.y}; }
DI u32x4 ldsr128(u32 addr) { return *(LAS u32x4*)addr; }
DI void ldsw128(u32 addr, u32x4 v) { *(LAS u32x4*)addr = v; }
DI rsrc_t mkrs(const void* p, u32 bytes) { return __builtin_amdgcn_make_buffer_rsrc((void*)p, 0, bytes, 0x00020000); }
DI u32x4 ld128c(rsrc_t r, u32 vo, u32 so) { return __builtin_amdgcn_raw_buffer_load_b128(r, vo, so, 16); }
DI void st128c(rsrc_t r, u32 vo, u32x4 v) { __builtin_amdgcn_raw_buffer_store_b128(v, r, vo, 0, 16); }
DI void glds(rsrc_t r, u32 vo, u32 so, u32 lds) {
  asm volatile("s_mov_b32 m0, %3\n\ts_nop 0\n\tbuffer_load_dwordx4 %0, %1, %2 offen lds" ::"v"(vo), "s"(r), "s"(so), "s"(lds) : "memory");
}
#define VMCNT(n) asm volatile("s_waitcnt vmcnt(%0)" ::"i"(n) : "memory")
#define BARRIER() asm volatile("s_waitcnt lgkmcnt(0)\n\ts_barrier" ::: "memory")
template <int CTRL> DI float dppf(float v) { return __uint_as_float(__builtin_amdgcn_update_dpp(0u, __float_as_uint(v), CTRL, 0xf, 0xf, true)); }
DI float sum16(float v) { v += dppf<0xB1>(v); v += dppf<0x4E>(v); v += dppf<0x141>(v); v += dppf<0x140>(v); return v; }
DI float xsum(float v) {
  auto a = __builtin_amdgcn_permlane16_swap(__float_as_uint(v), __float_as_uint(v), false, false);
  v = __uint_as_float(a[0]) + __uint_as_float(a[1]);
  auto b = __builtin_amdgcn_permlane32_swap(__float_as_uint(v), __float_as_uint(v), false, false);
  return __uint_as_float(b[0]) + __uint_as_float(b[1]);
}
DI float xmax(float v) {
  auto a = __builtin_amdgcn_permlane16_swap(__float_as_uint(v), __float_as_uint(v), false, false);
  v = fmaxf(__uint_as_float(a[0]), __uint_as_float(a[1]));
  auto b = __builtin_amdgcn_permlane32_swap(__float_as_uint(v), __float_as_uint(v), false, false);
  return fmaxf(__uint_as_float(b[0]), __uint_as_float(b[1]));
}
DI float dot2(u32 a, u32 b, float c) { return __builtin_amdgcn_fdot2_f32_bf16(__builtin_bit_cast(bf16x2, a), __builtin_bit_cast(bf16x2, b), c, false); }
DI float sum32(float v) {
  v = sum16(v);
  auto a = __builtin_amdgcn_permlane16_swap(__float_as_uint(v), __float_as_uint(v), false, false);
  return __uint_as_float(a[0]) + __uint_as_float(a[1]);
}
#define SB() __builtin_amdgcn_sched_barrier(0)
DI u32 opq(u32 v) { asm volatile("" : "+s"(v)); return v; }
DI u32 opv(u32 v) { asm volatile("" : "+v"(v)); return v; }
#define PIN(x) asm volatile("" : "+v"(x))
template <int V> struct IC { static constexpr int v = V; };
template <int I, int N, class Fn> DI void sfor(Fn&& f) { if constexpr (I < N) { f(IC<I>{}); sfor<I + 1, N>(f); } }
DI u32 cw_ld(u32 addr) {
  u32 v; asm volatile("ds_read_b32 %0, %1\n\ts_waitcnt lgkmcnt(0)" : "=v"(v) : "v"(addr) : "memory");
  return __builtin_amdgcn_readfirstlane(v);
}
DI u32 cw_min4(u32 addr) {
  u32x4 v; asm volatile("ds_read_b128 %0, %1\n\ts_waitcnt lgkmcnt(0)" : "=v"(v) : "v"(addr) : "memory");
  return __builtin_amdgcn_readfirstlane(min(min(v.x, v.y), min(v.z, v.w)));
}
DI void cw_st(u32 addr, u32 val, int lane) {
  if (lane == 0) asm volatile("ds_write_b32 %0, %1" ::"v"(addr), "v"(val) : "memory");
}
DI void wait_min4(u32 addr, u32 need) {
#pragma unroll 1
  for (u32 sp = 0; sp < SPIN_L; ++sp) { if (cw_min4(addr) >= need) break; __builtin_amdgcn_s_sleep(1); }
}
DI void wait_word(u32 addr, u32 need) {
#pragma unroll 1
  for (u32 sp = 0; sp < SPIN_L; ++sp) { if (cw_ld(addr) >= need) break; __builtin_amdgcn_s_sleep(1); }
}
DI u32 cw_add(u32 addr, int lane) {
  u32 old = 0;
  if (lane == 0) asm volatile("ds_add_rtn_u32 %0, %1, %2\n\ts_waitcnt lgkmcnt(0)" : "=v"(old) : "v"(addr), "v"(1u) : "memory");
  return __builtin_amdgcn_readfirstlane(old);
}
DI void flag_set(char* ws, u32 base, int id, u64 tg, int lane) {
  if (lane < 8) __hip_atomic_store((GAS u64*)(ws + base + (lane * 256 + id) * 16), tg, __ATOMIC_RELAXED, __HIP_MEMORY_SCOPE_AGENT);
}
template <int N> DI void flag_wait(char* ws, u32 base, int rep, int id0, int id1, u64 tag) {
  GAS u64* p0 = (GAS u64*)(ws + base + (rep * 256 + id0) * 16);
  GAS u64* p1 = (GAS u64*)(ws + base + (rep * 256 + id1) * 16);
#pragma unroll 1
  for (u32 sp = 0; sp < SPIN_G; ++sp) {
    bool ok = __hip_atomic_load(p0, __ATOMIC_RELAXED, __HIP_MEMORY_SCOPE_AGENT) == tag;
    if (N > 1) ok &= __hip_atomic_load(p1, __ATOMIC_RELAXED, __HIP_MEMORY_SCOPE_AGENT) == tag;
    if (__all(ok)) break;
    __builtin_amdgcn_s_sleep(2);
  }
  __builtin_amdgcn_fence(__ATOMIC_ACQUIRE, "wavefront");
  asm volatile("" ::: "memory");
}
DI void publish(char* ws, u32 base, int id, u64 tag, u32 cnt, u32 nw, int lane) {
  VMCNT(0);
  if (cw_add(cnt, lane) == nw - 1) flag_set(ws, base, id, tag, lane);
}

#define NSTAMP 64
#define ST(i) do { u64 t_; asm volatile("s_memrealtime %0\n\ts_waitcnt lgkmcnt(0)" : "=s"(t_) :: "memory"); u32 t2_ = (u32)t_ | 1u; asm volatile("v_writelane_b32 %0, %1, %2" : "+v"(stampv) : "s"(t2_), "i"(i)); } while (0)

struct Ctx {
  const u16 *x, *wqkv, *wphi, *wo, *w1, *b1, *w2, *b2;
  u16* out;
  char* ws;
};

DI u32x4 hedgehog(f32x4 p) {
  const float L2E = 1.4426950408889634f;
  u32 a = pk2(p[0], p[1]), b = pk2(p[2], p[3]);
  float p0 = lo16(a) * L2E, p1 = hi16(a) * L2E, p2 = lo16(b) * L2E, p3 = hi16(b) * L2E;
  float mx = xmax(fmaxf(fmaxf(p0, p1), fmaxf(p2, p3)));
  float mn = -xmax(-fminf(fminf(p0, p1), fminf(p2, p3)));
  float e0 = __builtin_amdgcn_exp2f(p0 - mx), e1 = __builtin_amdgcn_exp2f(p1 - mx), e2 = __builtin_amdgcn_exp2f(p2 - mx), e3 = __builtin_amdgcn_exp2f(p3 - mx);
  float n0 = __builtin_amdgcn_exp2f(mn - p0), n1 = __builtin_amdgcn_exp2f(mn - p1), n2 = __builtin_amdgcn_exp2f(mn - p2), n3 = __builtin_amdgcn_exp2f(mn - p3);
  float rs = __builtin_amdgcn_rcpf(xsum((e0 + e1) + (e2 + e3)));
  float rn = __builtin_amdgcn_rcpf(xsum((n0 + n1) + (n2 + n3)));
  return u32x4{pk2(e0 * rs, e1 * rs), pk2(e2 * rs, e3 * rs), pk2(n0 * rn, n1 * rn), pk2(n2 * rn, n3 * rn)};
}

constexpr int c_need(int n) { return n < 9 ? (n == 0 ? 44 : n <= 2 ? 48 : n == 3 ? 52 : n <= 5 ? 56 : n == 6 ? 60 : 64) : 64 + 4 * (n - 8); }

__global__ __launch_bounds__(512, 2) void fused(const u16* __restrict__ x, const u16* __restrict__ wqkv, const u16* __restrict__ wphi,
                                                const u16* __restrict__ wo, const u16* __restrict__ w1, const u16* __restrict__ b1,
                                                const u16* __restrict__ w2, const u16* __restrict__ b2, u16* __restrict__ out, char* ws) {
#if __HIP_DEVICE_COMPILE__
  u32 stampv = 0;
  ST(0);
  const int tid = threadIdx.x, lane = tid & 63, w = __builtin_amdgcn_readfirstlane(tid >> 6);
  const int l15 = lane & 15, g = lane >> 4;
  const int bid = blockIdx.x, xc = bid & 7, r = bid >> 3;
  const bool isG = w < 4;
  const int hw = w & 3;
  const int stile = 8 * (xc >> 1) + (r & 7), dtile = 4 * (xc & 1) + (r >> 3);
  const bool roleA = r < 16;
  const int j = r & 15;
  const int b_dt = 4 * (xc & 1) + (j & 3), b_ft = 4 * (xc >> 1) + (j >> 2);
  const bool roleB = !roleA && b_ft < 15;
  const int a_rb = 4 * (xc >> 1) + (j & 3), a_grp = 4 * (xc & 1) + (j >> 2);
  const rsrc_t rws = mkrs(ws, WS_END);
  const rsrc_t rx = mkrs(x, S * D * 2);

  const u64 epoch = __hip_atomic_load((GAS u64*)(ws + WS_EPOCH), __ATOMIC_RELAXED, __HIP_MEMORY_SCOPE_AGENT);
#define tag (epoch + 1)
  if (tid < 64) *(LAS u32*)(L_CTL + tid * 4) = 0;
  BARRIER();
  ST(12);

  const u32 cswz = (((lane & 7) ^ (((lane >> 4) & 3) << 1)) << 4);
  const int crot = CROT;
  auto c_issue = [&](int n) {
    const u32 dst = ((6 + n) % 9) * 16384 + hw * 4096;
    if (n < 11) {
      const rsrc_t rw = mkrs(w2, F * S * 2);
      int ks = 4 * n + hw; if (ks > 42) ks = 42;
      ks += crot; if (ks >= 43) ks -= 43;
      const u32 vo = (lane >> 3) * 4096 + cswz + stile * 128;
#pragma unroll
      for (int m = 0; m < 4; ++m) glds(rw, vo, ks * 131072 + m * 32768, dst + m * 1024);
    } else {
      const rsrc_t rw = mkrs(wo, D * D * 2);
      const int kt = 4 * (n - 11) + hw;
      const u32 vo = (lane >> 3) * 1024 + cswz + dtile * 128;
#pragma unroll
      for (int m = 0; m < 4; ++m) glds(rw, vo, kt * 32768 + m * 8192, dst + m * 1024);
    }
  };
  int c_first = 0;
  u32 fullv = 0;
  auto pub = [&](u32 v) { if (v > fullv) { fullv = v; cw_st(L_FULL + hw * 4, v, lane); } };

  if (roleA) {
    const int rq = w & 3, hp = w >> 2;
    const rsrc_t rw = mkrs(wqkv, D * 3 * D * 2);
    const int row0 = a_rb * 128;
    const u32 xvo = (row0 + 16 * w + (lane >> 3)) * 1024, xsw = lane >> 4;
    const u32 wvo = (lane >> 3) * 3072 + (((lane & 7) ^ ((((lane >> 4) & 1) | ((w & 1) << 1)) << 1)) << 4) + a_grp * 128;
    const u32 xv0 = xvo + (((lane & 7) ^ xsw) << 4), xv1 = xvo + (((lane & 7) ^ (xsw + 4)) << 4);
    auto a_issue1 = [&](int c, int i) {
      const u32 dst = (c % 3) * 40960;
      if (i < 2) glds(rx, i ? xv1 : xv0, i * 8192 + c * 128, dst + w * 2048 + i * 1024);
      else glds(rw, wvo, (64 * c + 8 * w) * 3072 + (i - 2) * 1024, dst + 16384 + (i - 2) * 8192 + w * 1024);
    };
    u32 wpr[2][4];
#pragma unroll
    for (int e = 0; e < 2; ++e) {
      const u16* p = wphi + (a_grp * 4 + 2 * hp + e) * 256 + 4 * g * 16 + l15;
#pragma unroll
      for (int i = 0; i < 4; ++i) wpr[e][i] = p[16 * i];
    }
    SB();
#pragma unroll
    for (int i = 0; i < 10; ++i) a_issue1(i / 5, i % 5);
    ST(13);
    f32x4 aq[2][2], ak[2][2], av[2][2];
#pragma unroll
    for (int a = 0; a < 2; ++a)
#pragma unroll
      for (int e = 0; e < 2; ++e) { aq[a][e] = f32x4{0, 0, 0, 0}; ak[a][e] = aq[a][e]; av[a][e] = aq[a][e]; }
    const u32 xb0 = (32 * rq + l15) * 128 + ((g ^ ((l15 >> 1) & 7)) << 4);
    const u32 wf = ((l15 >> 3) & 1) | ((g & 1) << 1);
    u32 wb[2];
#pragma unroll
    for (int e = 0; e < 2; ++e) wb[e] = 16384 + (8 * g + (l15 >> 2)) * 128 + (((2 * hp + e) ^ wf) << 5) + (l15 & 3) * 8;
    u32x4 xa[2][2], wa[2][6];
    auto a_step = [&](bool mma, int kc, bool rd, int kr, u32 sb, int cn, int p0, int np) {
      u32 xb = 0, wbb[2] = {0, 0};
      if (rd) { xb = opv(sb + (xb0 ^ (kr * 64))); wbb[0] = opv(sb + wb[0]); wbb[1] = opv(sb + wb[1]); }
#pragma unroll
      for (int q = 0; q < 6; ++q) {
        const int e = q / 3, sec = q % 3;
        if (rd) {
          if (q < 2) xa[kr][q] = ldsr128(xb + q * 2048);
          const u32 ad = wbb[e] + sec * 8192 + kr * 4096;
          wa[kr][q] = cat(trd(ad), trd(ad + 512));
        }
        SB();
        if (mma) {
#pragma unroll
          for (int a = 0; a < 2; ++a) {
            if (sec == 0) { aq[a][e] = mfma32(wa[kc][q], xa[kc][a], aq[a][e]); PIN(aq[a][e]); }
            if (sec == 1) { ak[a][e] = mfma32(wa[kc][q], xa[kc][a], ak[a][e]); PIN(ak[a][e]); }
            if (sec == 2) { av[a][e] = mfma32(xa[kc][a], wa[kc][q], av[a][e]); PIN(av[a][e]); }
          }
        }
        if ((q & 1) && q / 2 < np) a_issue1(cn, p0 + q / 2);
        SB();
      }
    };
    u64 early = 0;
    GAS u64* const pfl = (GAS u64*)(ws + (lane < 8 ? WS_FH + (xc * 256 + a_rb * 8 + lane) * 16 : WS_FCS + (xc * 256 + a_grp) * 16));
    u32x2 stl[2]; u32x4 stv[2][2], csq[2], csk[2]; u32 csv[2];
#pragma unroll
    for (int c = 0; c < 8; ++c) {
      if (c == 7 && w == 0) {
        VMCNT(0);
        asm volatile("" : "+v"(early));
        if (!__all(early == tag)) flag_wait<1>(ws, lane < 8 ? WS_FH : WS_FCS, xc, lane < 8 ? a_rb * 8 + lane : a_grp, 0, tag);
      }
      if (c < 7) VMCNT(5); else VMCNT(0);
      BARRIER();
      ST(16 + c);
      const u32 sb = (c % 3) * 40960;
      if (c == 7) {
#pragma unroll
        for (int a = 0; a < 2; ++a) {
          const u32 rb8 = WS_STAT + (row0 + 32 * rq + 16 * a) * 8;
          stl[a] = __builtin_amdgcn_raw_buffer_load_b64(rws, rb8 + l15 * 8, 0, 16);
          stv[a][0] = ld128c(rws, rb8 + g * 32, 0);
          stv[a][1] = ld128c(rws, rb8 + g * 32 + 16, 0);
        }
#pragma unroll
        for (int e = 0; e < 2; ++e) {
          const u32 cb = WS_CS + (a_grp * 64 + (2 * hp + e) * 16) * 4;
          csq[e] = ld128c(rws, cb + g * 16, 0);
          csk[e] = ld128c(rws, cb + 2048 + g * 16, 0);
          csv[e] = __builtin_amdgcn_raw_buffer_load_b32(rws, cb + 4096 + l15 * 4, 0, 16);
        }
      }
      if (c == 6 && w == 0) asm volatile("global_load_dwordx2 %0, %1, off sc1" : "=v"(early) : "v"(pfl) : "memory");
      a_step(c > 0, 1, true, 0, sb, c + 2, 0, c < 6 ? 2 : 0);
      a_step(true, 0, true, 1, sb, c + 2, 2, c < 6 ? 3 : 0);
    }
    a_step(true, 1, false, 0, 0, 0, 0, 0);
    u32x2 wph[2];
#pragma unroll
    for (int e = 0; e < 2; ++e) wph[e] = u32x2{wpr[e][0] | (wpr[e][1] << 16), wpr[e][2] | (wpr[e][3] << 16)};
    ST(2);
    BARRIER();
    const u32 one2 = 0x3f803f80u;
    u32x4 selP = {0, 0, 0, 0}, selN = {0, 0, 0, 0};
    {
      const int dlt = l15 - 4 * g;
      const u32 v0 = dlt == 0 ? 0x3f80u : dlt == 1 ? 0x3f800000u : 0u, v1 = dlt == 2 ? 0x3f80u : dlt == 3 ? 0x3f800000u : 0u;
      selP.x = v0; selP.y = v1; selN.z = v0; selN.w = v1;
    }
    auto fold = [&](f32x4 acc, float m, float ri, u32x4 cs) {
      return u32x2{pk2((acc[0] - m * __uint_as_float(cs.x)) * ri, (acc[1] - m * __uint_as_float(cs.y)) * ri),
                   pk2((acc[2] - m * __uint_as_float(cs.z)) * ri, (acc[3] - m * __uint_as_float(cs.w)) * ri)};
    };
    const f32x4 zero4 = {0, 0, 0, 0};
#pragma unroll
    for (int e = 0; e < 2; ++e) {
      u32x2 tp[2], tn[2], vb[2];
#pragma unroll
      for (int a = 0; a < 2; ++a) {
        const u32x4 ph = hedgehog(mfma16(wph[e], fold(ak[a][e], __uint_as_float(stl[a].x), __uint_as_float(stl[a].y), csk[e]), zero4));
        const f32x4 p4 = mfma32(ph, selP, zero4), n4 = mfma32(ph, selN, zero4);
        tp[a] = u32x2{pk2(p4[0], p4[1]), pk2(p4[2], p4[3])};
        tn[a] = u32x2{pk2(n4[0], n4[1]), pk2(n4[2], n4[3])};
        const float cv = __uint_as_float(csv[e]);
        const f32x4 t = av[a][e];
        vb[a] = u32x2{pk2((t[0] - __uint_as_float(stv[a][0].x) * cv) * __uint_as_float(stv[a][0].y), (t[1] - __uint_as_float(stv[a][0].z) * cv) * __uint_as_float(stv[a][0].w)),
                      pk2((t[2] - __uint_as_float(stv[a][1].x) * cv) * __uint_as_float(stv[a][1].y), (t[3] - __uint_as_float(stv[a][1].z) * cv) * __uint_as_float(stv[a][1].w))};
      }
      const u32x4 P = cat(tp[0], tp[1]), N = cat(tn[0], tn[1]), V = cat(vb[0], vb[1]), ones = {one2, one2, one2, one2};
      const f32x4 kvp = mfma32(P, V, zero4), kvn = mfma32(N, V, zero4), zp = mfma32(P, ones, zero4), zn = mfma32(N, ones, zero4);
      const int ti = (2 * hp + e) * 2;
      ldsw128(L_AKV + (ti * 4 + rq) * 1024 + lane * 16, __builtin_bit_cast(u32x4, kvp));
      ldsw128(L_AKV + ((ti + 1) * 4 + rq) * 1024 + lane * 16, __builtin_bit_cast(u32x4, kvn));
      if (l15 == 0) {
        ldsw128(L_AZ + (ti * 4 + rq) * 64 + g * 16, __builtin_bit_cast(u32x4, zp));
        ldsw128(L_AZ + ((ti + 1) * 4 + rq) * 64 + g * 16, __builtin_bit_cast(u32x4, zn));
      }
    }
    BARRIER();
    ST(3);
    if (isG) {
      f32x4 sp = zero4, sn = zero4;
      float zz = 0.f;
#pragma unroll
      for (int q = 0; q < 4; ++q) {
        sp += __builtin_bit_cast(f32x4, ldsr128(L_AKV + (w * 8 + q) * 1024 + lane * 16));
        sn += __builtin_bit_cast(f32x4, ldsr128(L_AKV + (w * 8 + 4 + q) * 1024 + lane * 16));
        zz += *(LAS float*)(L_AZ + (w * 8 + ((lane >> 4) & 1) * 4 + q) * 64 + l15 * 4);
      }
      const u32 rec = a_rb * 32 + a_grp * 4 + w;
      st128c(rws, WS_KV + rec * 1024 + lane * 16, u32x4{pk2(sp[0], sp[1]), pk2(sp[2], sp[3]), pk2(sn[0], sn[1]), pk2(sn[2], sn[3])});
      if (lane < 32) __builtin_amdgcn_raw_buffer_store_b32(__float_as_uint(zz), rws, WS_Z + rec * 128 + lane * 4, 0, 16);
      publish(ws, WS_FKV, a_rb * 8 + a_grp, tag, L_CNT, 4, lane);
    } else {
#pragma unroll
      for (int n = 0; n < 6; ++n) c_issue(n);
      c_first = 6;
    }
    ST(4);
    u32x4 phq[2][2];
#pragma unroll
    for (int a = 0; a < 2; ++a)
#pragma unroll
      for (int e = 0; e < 2; ++e)
        phq[a][e] = hedgehog(mfma16(wph[e], fold(aq[a][e], __uint_as_float(stl[a].x), __uint_as_float(stl[a].y), csq[e]), zero4));
    ST(5);
    if (isG) {
      if (w == 0) { flag_wait<1>(ws, WS_FKV, xc, (lane & 15) * 8 + a_grp, 0, tag); cw_st(L_LND, 1, lane); }
      else wait_word(L_LND, 1);
      ST(6);
      u32x4 kvl[16]; u32 zl[16];
#pragma unroll
      for (int q = 0; q < 16; ++q) {
        const u32 rec = q * 32 + a_grp * 4 + w;
        kvl[q] = ld128c(rws, WS_KV + lane * 16, rec * 1024);
        zl[q] = __builtin_amdgcn_raw_buffer_load_b32(rws, WS_Z + (lane & 31) * 4, rec * 128, 16);
      }
      SB();
      const u32 selo = opv(0x3f80u), sehi = opv(0x3f800000u);
      float a8[8], zz = 0.f;
#pragma unroll
      for (int i = 0; i < 8; ++i) a8[i] = 0.f;
#pragma unroll
      for (int q = 0; q < 16; ++q) {
#pragma unroll
        for (int i = 0; i < 4; ++i) { a8[2 * i] = dot2(kvl[q][i], selo, a8[2 * i]); a8[2 * i + 1] = dot2(kvl[q][i], sehi, a8[2 * i + 1]); }
        zz += __uint_as_float(zl[q]);
      }
      ldsw128(L_AT + w * 1024 + lane * 16, u32x4{pk2(a8[0], a8[1]), pk2(a8[2], a8[3]), pk2(a8[4], a8[5]), pk2(a8[6], a8[7])});
      if (lane < 32) *(LAS u16*)(L_AZT + w * 64 + lane * 2) = (u16)pk2(zz, zz);
    }
    BARRIER();
    ST(14);
    {
      u32x4 akv[2], az[2];
#pragma unroll
      for (int e = 0; e < 2; ++e) {
        akv[e] = ldsr128(L_AT + (2 * hp + e) * 1024 + lane * 16);
        az[e] = cat(*(LAS u32x2*)(L_AZT + (2 * hp + e) * 64 + g * 8), *(LAS u32x2*)(L_AZT + (2 * hp + e) * 64 + 32 + g * 8));
      }
#pragma unroll
      for (int a = 0; a < 2; ++a) {
        u32x2 ov[2];
#pragma unroll
        for (int e = 0; e < 2; ++e) {
          const f32x4 num = mfma32(akv[e], phq[a][e], zero4), den = mfma32(az[e], phq[a][e], zero4);
          const float rd = __builtin_amdgcn_rcpf(bfr(bfr(den[0]) + 1e-6f));
          const u32 n01 = pk2(num[0], num[1]), n23 = pk2(num[2], num[3]);
          ov[e] = u32x2{pk2(lo16(n01) * rd, hi16(n01) * rd), pk2(lo16(n23) * rd, hi16(n23) * rd)};
        }
        st128c(rws, WS_O + ((a_grp * 2 + hp) * 128 + a_rb * 8 + 2 * rq + a) * 1024 + lane * 16, cat(ov[0], ov[1]));
      }
      publish(ws, WS_FO, a_rb * 8 + a_grp, tag, L_CNT + 4, 8, lane);
      if (!isG) pub(16 + 6);
    }
    BARRIER();
    if (isG) cw_st(L_PROG + hw * 4, 64, lane);
    ST(7);
  } else {
    const int li = xc * 16 + j;
    const int rot = BROT;
    const rsrc_t rw1 = mkrs(w1, S * F * 2);
    u32 vo[6];
    u32x4 cst[24];
    auto b_issue = [&](int m) {
      const u32 so = ((4 * m + hw + rot) & 63) * (32 * F * 2);
#pragma unroll
      for (int n = 0; n < 6; ++n) glds(rw1, vo[n], so, (m % 6) * 24576 + hw * 6144 + n * 1024);
    };
    {
      const int rl = 2 * w + (lane >> 5), row = li * 16 + rl;
      u32x4 xr[2];
      asm volatile("buffer_load_dwordx4 %0, %2, %3, 0 offen\n\tbuffer_load_dwordx4 %1, %2, %3, 0 offen offset:512"
                   : "=&v"(xr[0]), "=&v"(xr[1]) : "v"(row * 1024 + (lane & 31) * 16), "s"(rx) : "memory");
      if (!isG && roleB) {
#pragma unroll
        for (int n = 0; n < 6; ++n) {
          const int c = n * 64 + lane, rr = c / 12;
          int ch = (c % 12) ^ (((rr >> 3) & 1) << 1);
          if (b_ft == 14) ch &= 3;
          vo[n] = rr * (F * 2) + ch * 16 + b_ft * 192;
        }
        b_issue(0);
        ST(13);
        VMCNT(6);
      } else if (!roleB) {
        const rsrc_t rw = mkrs(wqkv, D * 3 * D * 2);
#pragma unroll
        for (int i = 0; i < 24; ++i)
          cst[i] = __builtin_amdgcn_raw_buffer_load_b128(rw, (w * 64 + (lane >> 3)) * 3072 + ((xc & 1) * 4 + (j & 3)) * 128 + (lane & 7) * 16, (i / 3) * 8 * 3072 + (i % 3) * 1024, 0);
        SB();
        VMCNT(24);
      } else {
        ST(13);
        VMCNT(0);
      }
      asm volatile("" : "+v"(xr[0]), "+v"(xr[1]));
      ST(1);
      u32 pk[8] = {xr[0].x, xr[0].y, xr[0].z, xr[0].w, xr[1].x, xr[1].y, xr[1].z, xr[1].w};
#pragma unroll
      for (int pass = 0; pass < 2; ++pass) {
        float s1 = 0.f, s2 = 0.f;
#pragma unroll
        for (int i = 0; i < 8; ++i) { s1 = dot2(pk[i], 0x3f803f80u, s1); s2 = dot2(pk[i], pk[i], s2); }
        const float m = sum32(s1) * (1.f / D);
        const float var = bfr(fmaxf(sum32(s2) * (1.f / D) - m * m, 0.f));
        const float mb = bfr(m);
        const float ri = __builtin_amdgcn_rcpf(bfr(__builtin_amdgcn_sqrtf(bfr(var + 1e-5f))));
#pragma unroll
        for (int i = 0; i < 8; ++i) pk[i] = pk2((lo16(pk[i]) - mb) * ri, (hi16(pk[i]) - mb) * ri);
        if (pass == 0 && (lane & 31) == 0) *(LAS u32x2*)(L_LNS + rl * 8) = u32x2{__float_as_uint(mb), __float_as_uint(ri)};
      }
      const u32 wa = L_LN + rl * LN_PITCH + (rl >> 3) * 32 + (lane & 31) * 16;
      ldsw128(wa, u32x4{pk[0], pk[1], pk[2], pk[3]});
      ldsw128(wa + 512, u32x4{pk[4], pk[5], pk[6], pk[7]});
      cw_add(L_CNT + 8, lane);
      ST(14);
    }
    if (isG) {
      wait_word(L_CNT + 8, 8);
      ST(15);
#pragma unroll
      for (int rr = 0; rr < 4; ++rr) {
        const int item = w * 16 + rr * 4 + g, dt = item >> 1, oct = item & 1;
        const u32 ad = L_LN + (8 * oct + (l15 >> 2)) * LN_PITCH + oct * 32 + dt * 32 + (l15 & 3) * 8;
        const u32x4 hv = cat(trd(ad), trd(ad + 4 * LN_PITCH));
        st128c(rws, WS_H + (((li >> 1) * 32 + dt) * 4 + (li & 1) * 2 + oct) * 256 + l15 * 16, hv);
      }
      if (w == 0 && lane < 8) st128c(rws, WS_STAT + li * 128 + lane * 16, ldsr128(L_LNS + lane * 16));
      ST(2);
      VMCNT(0);
      if (cw_add(L_CNT + 12, lane) == 3) { flag_set(ws, WS_FH, li, tag, lane); cw_st(L_LND, 1, lane); }
      ST(3);
    }
    if (!roleB) {
      const int grp = (xc & 1) * 4 + (j & 3);
      const u32 sv = (l15 & 1) ? 0x3f800000u : 0x3f80u;
      const u32x4 sel = {l15 >> 1 == 0 ? sv : 0u, l15 >> 1 == 1 ? sv : 0u, l15 >> 1 == 2 ? sv : 0u, l15 >> 1 == 3 ? sv : 0u};
      f32x4 cs[3] = {{0, 0, 0, 0}, {0, 0, 0, 0}, {0, 0, 0, 0}};
#pragma unroll
      for (int i = 0; i < 24; ++i) cs[i % 3] = mfma32(cst[i], sel, cs[i % 3]);
#pragma unroll
      for (int sec = 0; sec < 3; ++sec) ldsw128((w * 3 + sec) * 1024 + lane * 16, __builtin_bit_cast(u32x4, cs[sec]));
      if (cw_add(L_CNT, lane) == 7) {
        const int pos = lane >> 3, jj = lane & 7;
        float o3[3];
#pragma unroll
        for (int sec = 0; sec < 3; ++sec) {
          float a = 0.f;
#pragma unroll
          for (int q = 0; q < 8; ++q) {
            const u32 ad = (q * 3 + sec) * 1024 + ((pos >> 2) * 16 + jj) * 16 + (pos & 3) * 4;
            a += *(LAS float*)ad + *(LAS float*)(ad + 512);
          }
          o3[sec] = a;
        }
#pragma unroll
        for (int sec = 0; sec < 3; ++sec) __builtin_amdgcn_raw_buffer_store_b32(__float_as_uint(o3[sec]), rws, WS_CS + sec * 2048 + grp * 256 + lane * 4, 0, 16);
        VMCNT(0);
        flag_set(ws, WS_FCS, grp, tag, lane);
        cw_st(L_CNT + 4, 1, lane);
        ST(4);
      }
    }
    if (isG) {
    if (roleB) {
      u32x2 bias[6];
#pragma unroll
      for (int t = 0; t < 6; ++t) { int f = b_ft * 96 + t * 16 + 4 * g; if (f > F - 4) f = F - 4; bias[t] = *(const GAS u32x2*)(b1 + f); }
      if (w == 0) {
        flag_wait<2>(ws, WS_FH, xc, lane, lane + 64, tag);
        cw_st(L_GO, 1, lane);
      } else {
        wait_word(L_GO, 1);
      }
      ST(4);
      const u32 hvo = (b_dt * 4 + w) * 1024 + lane * 16;
      constexpr int P = 16;
      u32x4 hr[P];
#pragma unroll
      for (int i = 0; i < P; ++i) hr[i] = ld128c(rws, hvo, WS_H + ((i + rot) & 63) * 32768);
      __builtin_amdgcn_sched_barrier(0);
      f32x4 acc[6];
#pragma unroll
      for (int t = 0; t < 6; ++t) acc[t] = f32x4{0, 0, 0, 0};
      const u32 rowb = (8 * g + (l15 >> 2)) * 192 + (l15 & 3) * 8;
      const u32 bE = rowb + (g & 1) * 32, bO = rowb - (g & 1) * 32;
      u32x4 wfr[2][6];
      u32 avail = 0;
      auto b_need = [&](u32 need) { if (avail < need) { wait_min4(L_FULL, need); avail = cw_min4(L_FULL); } };
      u32 gE = bE, gO = bO;
      auto b_rd = [&](int buf, int k, int t) {
        const u32 ad = ((t & 1) ? gO : gE) + k * 6144 + t * 32;
        wfr[buf][t] = cat(trd(ad), trd(ad + 768));
      };
      b_need(1);
#pragma unroll
      for (int t = 0; t < 6; ++t) b_rd(0, 0, t);
      u32 pos = 0;
#pragma unroll 1
      for (int mo = 0; mo < 4; ++mo) {
        const u32 hv2 = mo < 3 ? hvo : 0x80000000u;
#pragma unroll
        for (int mi = 0; mi < 4; ++mi) {
          const int m = mo * 4 + mi;
          ST(16 + mi);
#pragma unroll
          for (int k = 0; k < 4; ++k) {
            if (k == 3) {
              cw_st(L_PROG + w * 4, 4 * (m + 1), lane);
              pos = pos == 5 ? 0 : pos + 1; gE = opv(bE + pos * 24576); gO = opv(bO + pos * 24576);
              b_need(m < 15 ? m + 2 : 0);
            }
#pragma unroll
            for (int t = 0; t < 6; ++t) {
              b_rd((k + 1) & 1, (k + 1) & 3, t);
              SB();
              acc[t] = mfma32(wfr[k & 1][t], hr[mi * 4 + k], acc[t]);
              PIN(acc[t]);
              SB();
            }
            hr[mi * 4 + k] = ld128c(rws, hv2, WS_H + ((m * 4 + k + P + rot) & 63) * 32768);
            SB();
          }
        }
      }
      ST(5);
#pragma unroll
      for (int m3 = 0; m3 < 3; ++m3) {
        u32x4 uv;
#pragma unroll
        for (int h2 = 0; h2 < 2; ++h2) {
          const int t = 2 * m3 + h2;
          float o4[4];
#pragma unroll
          for (int i = 0; i < 4; ++i) {
            const u32 bw = i < 2 ? bias[t].x : bias[t].y;
            const float v = acc[t][i] + ((i & 1) ? hi16(bw) : lo16(bw));
            const float zz = v * (1.f + 0.044715f * v * v) * (-2.f * 0.7978845608028654f * 1.4426950408889634f);
            o4[i] = v * __builtin_amdgcn_rcpf(1.f + __builtin_amdgcn_exp2f(zz));
          }
          uv[2 * h2] = pk2(o4[0], o4[1]); uv[2 * h2 + 1] = pk2(o4[2], o4[3]);
        }
        if (b_ft * 3 + m3 < 43) st128c(rws, WS_U + ((b_ft * 3 + m3) * 32 + b_dt * 4 + w) * 1024 + lane * 16, uv);
      }
      ST(6);
      publish(ws, WS_FU, b_dt * 16 + b_ft, tag, L_CNT, 4, lane);
      ST(7);
    } else {
      wait_word(L_LND, 1);
      cw_st(L_PROG + w * 4, 64, lane);
    }
    } else if (roleB) {
#pragma unroll
    for (int m = 1; m < 16; ++m) {
      if (m == 5) wait_word(L_LND, 1);
      if (m >= 6) {
        if (cw_min4(L_PROG) < 4 * (m - 5)) { VMCNT(0); pub(m); wait_min4(L_PROG, 4 * (m - 5)); }
      }
      b_issue(m);
      if (m >= 3) { VMCNT(18); pub(m - 2); }
      ST(16 + m);
    }
    VMCNT(12); pub(14); VMCNT(6); pub(15); VMCNT(0); pub(16);
    ST(4);
  } else {
    wait_word(L_CNT + 4, 1);
    }
  }

  if (isG) {
    const int srow = stile * 64 + l15;
    u32x2 xin[4]; u32 b2v[4];
#pragma unroll
    for (int t = 0; t < 4; ++t) {
      xin[t] = *(const GAS u32x2*)(x + (srow + 16 * t) * D + dtile * 64 + w * 16 + 4 * g);
      b2v[t] = b2[srow + 16 * t];
    }
    if (w == 0) {
      flag_wait<1>(ws, WS_FU, xc, dtile * 16 + (l15 < 15 ? l15 : 0), 0, tag);
      cw_st(L_GO, 2, lane);
    } else {
      wait_word(L_GO, 2);
    }
    ST(8);
    constexpr int P = 16;
    const u32 uvo = (dtile * 4 + w) * 1024 + lane * 16, ovo = (stile * 4 + w) * 1024 + lane * 16;
    auto uoff = [&](int i) { int ks = i + crot; if (ks >= 43) ks -= 43; return WS_U + ks * 32768; };
    u32x4 ar[P];
#pragma unroll
    for (int i = 0; i < P; ++i) ar[i] = ld128c(rws, uvo, uoff(i));
    __builtin_amdgcn_sched_barrier(0);
    f32x4 ay[4], as[4];
#pragma unroll
    for (int t = 0; t < 4; ++t) { ay[t] = f32x4{0, 0, 0, 0}; as[t] = ay[t]; }
    u32 tb[4];
    {
      const int row = 4 * g + (l15 >> 2), f = (row >> 1) & 3;
#pragma unroll
      for (int t = 0; t < 4; ++t) tb[t] = row * 128 + ((t ^ f) << 5) + (l15 & 3) * 8;
    }
    u32x4 wfr[3][4];
    u32 avail = 0;
    auto c_needf = [&](u32 need) { if (avail < need) { wait_min4(L_FULL, need); avail = cw_min4(L_FULL); } };
    u32 tg[4];
    auto c_pre = [&](auto ic) {
      constexpr int i = decltype(ic)::v;
      if (i < 60 && (i & 3) == 0) {
        c_needf(16 + i / 4 + 1);
#pragma unroll
        for (int t = 0; t < 4; ++t) tg[t] = opv(tb[t] + ((6 + i / 4) % 9) * 16384);
      }
    };
    auto c_rd = [&](auto ic, int t) {
      constexpr int i = decltype(ic)::v;
      if (i == 43 || i >= 60) return;
      const u32 ad = tg[t] + (i & 3) * 4096;
      wfr[i % 3][t] = cat(trd(ad), trd(ad + 2048));
    };
    c_pre(IC<0>{});
#pragma unroll
    for (int t = 0; t < 4; ++t) { c_rd(IC<0>{}, t); }
#pragma unroll
    for (int t = 0; t < 4; ++t) { c_rd(IC<1>{}, t); }
    sfor<0, 60>([&](auto ic) {
      constexpr int i = decltype(ic)::v;
      if ((i & 3) == 0) ST(32 + i / 4);
      if (i == 36) {
        wait_word(L_GOO, 1);
        ST(9);
#pragma unroll
        for (int q = 44; q < 52; ++q) ar[q % P] = ld128c(rws, ovo, WS_O + (q - 44) * 131072);
      }
      c_pre(IC<i + 2>{});
#pragma unroll
      for (int t = 0; t < 4; ++t) {
        c_rd(IC<i + 2>{}, t);
        SB();
        if (i < 43) { ay[t] = mfma32(ar[i % P], wfr[i % 3][t], ay[t]); PIN(ay[t]); }
        if (i > 43) { as[t] = mfma32(wfr[i % 3][t], ar[i % P], as[t]); PIN(as[t]); }
        SB();
      }
      if (((i + 2) & 3) == 3 && i + 2 < 60) cw_st(L_PROG + w * 4, 64 + 4 * ((i + 2) / 4 + 1), lane);
      constexpr int nx = i + P;
      if (nx < 43) ar[i % P] = ld128c(rws, uvo, uoff(nx));
      else if (nx >= 52 && nx < 60) ar[i % P] = ld128c(rws, ovo, WS_O + (nx - 44) * 131072);
      SB();
    });
    ST(10);
#pragma unroll
    for (int t = 0; t < 4; ++t)
      if (t != w) ldsw128(L_SCR + (t * 4 + w) * 1024 - (w > t ? 1024 : 0) - t * 1024 + lane * 16, __builtin_bit_cast(u32x4, as[t]));
    BARRIER();
#pragma unroll
    for (int t = 0; t < 4; ++t) {
      f32x4 sv;
      if (t == w) sv = w == 0 ? as[0] : w == 1 ? as[1] : w == 2 ? as[2] : as[3];
      else sv = __builtin_bit_cast(f32x4, ldsr128(L_SCR + (w * 3 + t - (t > w ? 1 : 0)) * 1024 + lane * 16));
      const float bb = lo16(b2v[t]);
      float o4[4];
#pragma unroll
      for (int i = 0; i < 4; ++i) {
        const float yy = ay[t][i] + bb;
        const float gg = yy * __builtin_amdgcn_rcpf(1.f + __builtin_amdgcn_exp2f(yy * -1.4426950408889634f));
        const u32 xw = i < 2 ? xin[t].x : xin[t].y;
        o4[i] = ((i & 1) ? hi16(xw) : lo16(xw)) + sv[i] * gg;
      }
      *(GAS u32x2*)(out + (srow + 16 * t) * D + dtile * 64 + w * 16 + 4 * g) = u32x2{pk2(o4[0], o4[1]), pk2(o4[2], o4[3])};
    }
  } else {
#pragma unroll
    for (int n = 0; n < 15; ++n) {
      if (n >= c_first) {
        if (cw_min4(L_PROG) < c_need(n)) { VMCNT(0); pub(16 + n); wait_min4(L_PROG, c_need(n)); }
        c_issue(n);
        VMCNT(12);
        pub(16 + n - 2);
        ST(32 + n);
      }
    }
    VMCNT(0);
    pub(31);
    ST(8);
    if (w == 4) {
      flag_wait<1>(ws, WS_FO, xc, (stile >> 1) * 8 + (lane & 7), 0, tag);
      cw_st(L_GOO, 1, lane);
      if (bid == 0 && lane == 0) __hip_atomic_store((GAS u64*)(ws + WS_EPOCH), tag, __ATOMIC_RELAXED, __HIP_MEMORY_SCOPE_AGENT);
    }
    ST(9);
    BARRIER();
  }

  if (bid == AMP_BID && w == AMP_W) {
    u32 dlt = AMP ? (u32)__builtin_amdgcn_readlane(stampv, AMP_B) - (u32)__builtin_amdgcn_readlane(stampv, AMP_A) : 0;
    if (dlt > 5000) dlt = 5000;
    u64 t0 = __builtin_amdgcn_s_memrealtime();
    while (__builtin_amdgcn_s_memrealtime() - t0 < (u64)dlt * 20) {}
  }
#endif
}
#undef tag
}

extern "C" void kernel_launch(void* const* d_in, const int* in_sizes, int n_in, void* d_out, int out_size, void* d_ws, size_t ws_size,
                              hipStream_t stream) {
  using namespace hk;
  (void)hipFuncSetAttribute((const void*)fused, hipFuncAttributeMaxDynamicSharedMemorySize, LDS_BYTES);
  fused<<<256, 512, LDS_BYTES, stream>>>((const u16*)d_in[0], (const u16*)d_in[1], (const u16*)d_in[2], (const u16*)d_in[3], (const u16*)d_in[5],
                                         (const u16*)d_in[6], (const u16*)d_in[7], (const u16*)d_in[8], (u16*)d_out, (char*)d_ws);
}
```

```cpp
#include <hip/hip_runtime.h>
#include <stdint.h>

namespace hk {
typedef unsigned short u16;
typedef unsigned u32;
typedef unsigned long long u64;
typedef u32 u32x4 __attribute__((ext_vector_type(4)));
typedef u32 u32x2 __attribute__((ext_vector_type(2)));
typedef float f32x4 __attribute__((ext_vector_type(4)));
typedef float f32x2 __attribute__((ext_vector_type(2)));
typedef float f32x16 __attribute__((ext_vector_type(16)));
typedef __bf16 bf16x8 __attribute__((ext_vector_type(8)));
typedef __bf16 bf16x2 __attribute__((ext_vector_type(2)));
typedef short s16x4 __attribute__((ext_vector_type(4)));
#define LAS __attribute__((address_space(3)))
#define GAS __attribute__((address_space(1)))
#define DI __device__ __forceinline__
typedef __amdgpu_buffer_rsrc_t rsrc_t;

#define NT 0
#define PAUSE 0
#define APRE2 APRE
#define APRIO 0
#define DELAYA 0
#define DELAYB 0
#define CSTAGE 0
#define CHOLD 48
#define HOLDN 6
#define AFIRST 1
#define PTOUCH 1
#define PSLEEP 1
#define NTW 0
#define NTX 2
#define NTW1 0
#define NTW2 0
#define BDEP 3
#define APRE 4
#define MAPB 0
#define HPRE 6
#define MAPC 1
constexpr int S = 2048, D = 512, F = 1376;
constexpr u32 WS_EPOCH = 0;
#define FSTR 128
constexpr u32 FL_SZ = 8 * 128 * 128;
constexpr u32 WS_FH = 4096, WS_FKV = WS_FH + FL_SZ, WS_FO = WS_FKV + FL_SZ, WS_FU = WS_FO + FL_SZ, WS_FP = WS_FH + 128 * 128;
constexpr int ID_HALL = 127;
constexpr u32 WS_STAT = WS_FU + FL_SZ;
constexpr u32 WS_Z = WS_STAT + S * 8;
constexpr u32 WS_KV = WS_Z + 16 * 32 * 128;
constexpr u32 WS_H = WS_KV + 16 * 32 * 1024;
constexpr u32 WS_O = WS_H + S * D * 2;
constexpr u32 WS_U = WS_O + S * D * 2;
constexpr u32 WS_P = WS_U + D * F * 2;
constexpr u32 WS_END = WS_P + 120 * 24576;
constexpr u32 WS_STAMP = 64u << 20;
constexpr u32 RING = 147456, L_SCR = RING, L_CTL = 163840 - 256, LDS_BYTES = 163840;
constexpr u32 L_FULL = L_CTL, L_PROG = L_CTL + 16, L_GO = L_CTL + 32, L_GOO = L_CTL + 36, L_LND = L_CTL + 40, L_CNT = L_CTL + 48, L_SYN = L_CTL + 96;
constexpr u32 L_LN = 122880, LN_PITCH = 1088, L_LNS = L_LN + 16 * LN_PITCH, L_DUMMY = L_CTL - 256;
constexpr u32 L_ACS = L_CTL - 1024, L_AST = L_SCR + 8192;
constexpr u32 L_AKV = 49152, L_AT = L_AKV + 8 * 4352, L_AZT = L_AT + 4096;
constexpr u32 L_BX = 49152, L_CY = 65536, L_CS = L_SCR;
constexpr u32 CODE_BYTES = 36 * 1024;
constexpr u32 SPIN_G = 1u << 15, SPIN_L = 1u << 20;

DI f32x16 mfma(u32x4 a, u32x4 b, f32x16 c) {
  return __builtin_amdgcn_mfma_f32_32x32x16_bf16(__builtin_bit_cast(bf16x8, a), __builtin_bit_cast(bf16x8, b), c, 0, 0, 0);
}
DI u32 pk2(float a, float b) { f32x2 v = {a, b}; return __builtin_bit_cast(u32, __builtin_convertvector(v, bf16x2)); }
DI float lo16(u32 p) { return __uint_as_float(p << 16); }
DI float hi16(u32 p) { return __uint_as_float(p & 0xffff0000u); }
DI float bfr(float a) { return lo16(pk2(a, a)); }
template <int O> DI u32x4 pk8(const f32x16& v) { return u32x4{pk2(v[O], v[O + 1]), pk2(v[O + 2], v[O + 3]), pk2(v[O + 4], v[O + 5]), pk2(v[O + 6], v[O + 7])}; }
template <int O> DI u32x4 get4(const f32x16& v) { return __builtin_bit_cast(u32x4, f32x4{v[O], v[O + 1], v[O + 2], v[O + 3]}); }
DI u32x2 trd(u32 addr) { return __builtin_bit_cast(u32x2, __builtin_amdgcn_ds_read_tr16_b64_v4i16((LAS s16x4*)addr)); }
DI u32x4 cat(u32x2 a, u32x2 b) { return u32x4{a.x, a.y, b.x, b.y}; }
DI u32x4 ldsr128(u32 addr) { return *(LAS u32x4*)addr; }
DI f32x4 ldsf4(u32 addr) { return *(LAS f32x4*)addr; }
DI void ldsw128(u32 addr, u32x4 v) { *(LAS u32x4*)addr = v; }
DI rsrc_t mkrs(const void* p, u32 bytes) { return __builtin_amdgcn_make_buffer_rsrc((void*)p, 0, bytes, 0x00020000); }
DI u32x4 ld128c(rsrc_t r, u32 vo, u32 so) { return __builtin_amdgcn_raw_buffer_load_b128(r, vo, so, 16); }
DI void st128c(rsrc_t r, u32 vo, u32x4 v) { __builtin_amdgcn_raw_buffer_store_b128(v, r, vo, 0, 16); }
template <bool STREAM> DI void glds(rsrc_t r, u32 vo, u32 so, u32 lds) {
  if (STREAM && NT) asm volatile("s_mov_b32 m0, %3\n\ts_nop 0\n\tbuffer_load_dwordx4 %0, %1, %2 offen nt lds" ::"v"(vo), "s"(r), "s"(so), "s"(lds) : "memory");
  else asm volatile("s_mov_b32 m0, %3\n\ts_nop 0\n\tbuffer_load_dwordx4 %0, %1, %2 offen lds" ::"v"(vo), "s"(r), "s"(so), "s"(lds) : "memory");
}
#define VMCNT(n) asm volatile("s_waitcnt vmcnt(%0)" ::"i"(n) : "memory")
#define BARRIER() asm volatile("s_waitcnt lgkmcnt(0)\n\ts_barrier" ::: "memory")
#define SB() __builtin_amdgcn_sched_barrier(0)
#define PIN(x) asm volatile("" : "+v"(x))
DI u32 opv(u32 v) { PIN(v); return v; }
template <int CTRL> DI float dppf(float v) { return __uint_as_float(__builtin_amdgcn_update_dpp(0u, __float_as_uint(v), CTRL, 0xf, 0xf, true)); }
DI float fmax2(float a, float b) { return __builtin_elementwise_maximum(a, b); }
DI float fmin2(float a, float b) { return __builtin_elementwise_minimum(a, b); }
DI f32x4 mfma16(u32x4 a, u32x4 b, f32x4 c) {
  return __builtin_amdgcn_mfma_f32_16x16x32_bf16(__builtin_bit_cast(bf16x8, a), __builtin_bit_cast(bf16x8, b), c, 0, 0, 0);
}
DI f32x2 pfma(f32x2 a, f32x2 b, f32x2 c) { return __builtin_elementwise_fma(a, b, c); }
DI f32x2 sp2(float a) { return f32x2{a, a}; }
DI f32x2 ex2(f32x2 a) { return f32x2{__builtin_amdgcn_exp2f(a.x), __builtin_amdgcn_exp2f(a.y)}; }
DI f32x2 rc2(f32x2 a) { return f32x2{__builtin_amdgcn_rcpf(a.x), __builtin_amdgcn_rcpf(a.y)}; }
DI u32 pk2v(f32x2 v) { return __builtin_bit_cast(u32, __builtin_convertvector(v, bf16x2)); }
DI f32x2 unpk(u32 p) { return f32x2{lo16(p), hi16(p)}; }
template <int I> DI f32x2 g2(const f32x16& v) { return f32x2{v[2 * I], v[2 * I + 1]}; }
DI void swap16(float& a, float& b) { auto r = __builtin_amdgcn_permlane16_swap(__float_as_uint(a), __float_as_uint(b), false, false); a = __uint_as_float(r[0]); b = __uint_as_float(r[1]); }
DI void swap32(float& a, float& b) { auto r = __builtin_amdgcn_permlane32_swap(__float_as_uint(a), __float_as_uint(b), false, false); a = __uint_as_float(r[0]); b = __uint_as_float(r[1]); }
DI float sum16(float v) { v += dppf<0xB1>(v); v += dppf<0x4E>(v); v += dppf<0x141>(v); v += dppf<0x140>(v); return v; }
template <bool MAX> DI void xred2(float& a, float& b) {
  swap32(a, b);
  a = MAX ? fmax2(a, b) : a + b;
  b = a; swap32(a, b);
}
DI float dot2(u32 a, u32 b, float c) { return __builtin_amdgcn_fdot2_f32_bf16(__builtin_bit_cast(bf16x2, a), __builtin_bit_cast(bf16x2, b), c, false); }
template <int V> struct IC { static constexpr int v = V; };
template <int I, int N, class Fn> DI void sfor(Fn&& f) { if constexpr (I < N) { f(IC<I>{}); sfor<I + 1, N>(f); } }
DI void cw_st(u32 addr, u32 val) { asm volatile("ds_write_b32 %0, %1" ::"v"(addr), "v"(val) : "memory"); }
template <bool SLEEP> DI void lds_wait(u32 addr, u32 need) {
  u32 v, cnt = SPIN_L;
  if (SLEEP)
    asm volatile("s_branch 2f\n1:\n\ts_sleep 2\n2:\n\tds_read_b32 %0, %2\n\ts_waitcnt lgkmcnt(0)\n\tv_cmp_gt_u32 vcc, %3, %0\n\ts_sub_u32 %1, %1, 1\n\t"
                 "s_cselect_b64 vcc, 0, vcc\n\ts_cmp_lg_u64 vcc, 0\n\ts_cbranch_scc1 1b" : "=&v"(v), "+s"(cnt) : "v"(addr), "s"(need) : "vcc", "scc", "memory");
  else
    asm volatile("1:\n\tds_read_b32 %0, %2\n\ts_waitcnt lgkmcnt(0)\n\tv_cmp_gt_u32 vcc, %3, %0\n\ts_sub_u32 %1, %1, 1\n\t"
                 "s_cselect_b64 vcc, 0, vcc\n\ts_cmp_lg_u64 vcc, 0\n\ts_cbranch_scc1 1b" : "=&v"(v), "+s"(cnt) : "v"(addr), "s"(need) : "vcc", "scc", "memory");
}
DI bool lds_test(u32 addr, u32 need) {
  u32 v; asm volatile("ds_read_b32 %0, %1\n\ts_waitcnt lgkmcnt(0)" : "=v"(v) : "v"(addr) : "memory");
  return __ballot(v < need) == 0;
}
DI void wait_word(u32 addr, u32 need) { lds_wait<true>(addr, need); }
DI u32 cw_add(u32 addr, int lane) {
  u32 old = 0;
  if (lane == 0) asm volatile("ds_add_rtn_u32 %0, %1, %2\n\ts_waitcnt lgkmcnt(0)" : "=v"(old) : "v"(addr), "v"(1u) : "memory");
  return __builtin_amdgcn_readfirstlane(old);
}
template <int NREP = 8> DI void flag_set(char* ws, u32 base, int id, u64 tg, int lane) {
  if (lane < NREP) __hip_atomic_store((GAS u64*)(ws + base + (lane * 128 + id) * FSTR), tg, __ATOMIC_RELAXED, __HIP_MEMORY_SCOPE_AGENT);
}
template <int N> DI void flag_wait(char* ws, u32 base, int rep, int id0, int id1, u64 tg) {
  GAS u64* p0 = (GAS u64*)(ws + base + (rep * 128 + id0) * FSTR);
  GAS u64* p1 = (GAS u64*)(ws + base + (rep * 128 + id1) * FSTR);
#pragma unroll 1
  for (u32 sp = 0; sp < SPIN_G; ++sp) {
    bool ok = __hip_atomic_load(p0, __ATOMIC_RELAXED, __HIP_MEMORY_SCOPE_AGENT) == tg;
    if (N > 1) ok &= __hip_atomic_load(p1, __ATOMIC_RELAXED, __HIP_MEMORY_SCOPE_AGENT) == tg;
    if (__all(ok)) break;
    __builtin_amdgcn_s_sleep(PSLEEP);
  }
  __builtin_amdgcn_fence(__ATOMIC_ACQUIRE, "wavefront");
  asm volatile("" ::: "memory");
}
template <int NREP = 8> DI void publish(char* ws, u32 base, int id, u64 tg, u32 cnt, u32 nw, int lane) {
  VMCNT(0);
  if (cw_add(cnt, lane) == nw - 1) flag_set<NREP>(ws, base, id, tg, lane);
}

#define ST(i)
#define ST2(i)
#define ST2R(i)

DI void hedgehog(const f32x16& p, float sc, u32x4 (&pos)[2], u32x4 (&neg)[2]) {
  float mx[2], mn[2];
#pragma unroll
  for (int e = 0; e < 2; ++e) {
    const int o = 8 * e;
    mx[e] = fmax2(fmax2(fmax2(fmax2(p[o], p[o + 1]), p[o + 2]), fmax2(fmax2(p[o + 3], p[o + 4]), p[o + 5])), fmax2(p[o + 6], p[o + 7]));
    mn[e] = -fmin2(fmin2(fmin2(fmin2(p[o], p[o + 1]), p[o + 2]), fmin2(fmin2(p[o + 3], p[o + 4]), p[o + 5])), fmin2(p[o + 6], p[o + 7]));
  }
  xred2<true>(mx[0], mn[0]); xred2<true>(mx[1], mn[1]);
  f32x2 ep[8], en[8];
  sfor<0, 8>([&](auto ic) {
    constexpr int k = decltype(ic)::v;
    ep[k] = pfma(g2<k>(p), sp2(sc), sp2(-mx[k >> 2] * sc));
    en[k] = pfma(g2<k>(p), sp2(-sc), sp2(-mn[k >> 2] * sc));
  });
#pragma unroll
  for (int k = 0; k < 8; ++k) { ep[k] = ex2(ep[k]); en[k] = ex2(en[k]); }
#pragma unroll
  for (int e = 0; e < 2; ++e) {
    const int o = 4 * e;
    const f32x2 a = (ep[o] + ep[o + 1]) + (ep[o + 2] + ep[o + 3]), b = (en[o] + en[o + 1]) + (en[o + 2] + en[o + 3]);
    float sp = a.x + a.y, sn = b.x + b.y;
    xred2<false>(sp, sn);
    const f32x2 rs = sp2(__builtin_amdgcn_rcpf(sp)), rn = sp2(__builtin_amdgcn_rcpf(sn));
#pragma unroll
    for (int k = 0; k < 4; ++k) { pos[e][k] = pk2v(ep[o + k] * rs); neg[e][k] = pk2v(en[o + k] * rn); }
  }
}

constexpr int c_need(int n) { return n < 9 ? (n <= 2 ? 48 : n == 3 ? 52 : n <= 5 ? 56 : n == 6 ? 60 : 64) : 64 + 4 * (n - 8); }

__global__ __launch_bounds__(512, 2) void fused(const u16* __restrict__ x, const u16* __restrict__ wqkv, const u16* __restrict__ wphi,
                                                const u16* __restrict__ wo, const u16* __restrict__ w1, const u16* __restrict__ b1,
                                                const u16* __restrict__ w2, const u16* __restrict__ b2, u16* __restrict__ out, char* ws) {
#if __HIP_DEVICE_COMPILE__
  ST(0);
  u64 pc0; asm volatile("s_getpc_b64 %0" : "=s"(pc0));
  const int tid = threadIdx.x, lane = tid & 63, w = __builtin_amdgcn_readfirstlane(tid >> 6);
  const int l15 = lane & 15, l31 = lane & 31, hh = lane >> 5, blk = (lane >> 4) & 1;
  const int bid = blockIdx.x, xc = bid & 7, r = bid >> 3;
  const bool isG = w < 4;
  const int hw = w & 3;
  const int stile = MAPC ? 8 * (xc >> 1) + (r & 7) : 4 * xc + (r & 3), dtile = MAPC ? 4 * (xc & 1) + (r >> 3) : r >> 2;
  const bool roleA = xc < 4;
  const int b_dt = r & 3, b_kz = MAPB ? (r >> 2) & 1 : xc & 1, b_ft = MAPB ? 4 * (xc - 4) + (r >> 3) : 8 * ((xc >> 1) & 1) + (r >> 2);
  const int b_id = (b_ft * 4 + b_dt) * 2 + b_kz;
  const bool roleB = !roleA && b_ft < 15;
  const int a_rb = 8 * (xc >> 1) + (r & 7), a_grp = 4 * (xc & 1) + (r >> 3);
  const rsrc_t rws = mkrs(ws, WS_END);
  const rsrc_t rx = mkrs(x, S * D * 2);
  const f32x16 zero16 = {0, 0, 0, 0, 0, 0, 0, 0, 0, 0, 0, 0, 0, 0, 0, 0};

  const u64 epoch = __hip_atomic_load((GAS u64*)(ws + WS_EPOCH), __ATOMIC_RELAXED, __HIP_MEMORY_SCOPE_AGENT);
#define tag (epoch + 1)
  if (tid < 320) *(LAS u32*)(L_CTL - 1024 + tid * 4) = 0;
  BARRIER();
  ST(12);
  auto code_touch = [&]() {
    const rsrc_t rc = mkrs((const char*)(pc0 & ~255ull) + r * (CODE_BYTES / 32), w == 7 ? CODE_BYTES / 32 : 0);
    asm volatile("s_mov_b32 m0, %2\n\ts_nop 0\n\tbuffer_load_dword %0, %1, 0 offen lds" ::"v"(lane * 128), "s"(rc), "s"(L_DUMMY) : "memory");
  };
  const u32 cswz = ((lane & 7) ^ (blk << 2)) << 4;
  const int crot = MAPC ? ((((r >> 3) * 2 + (r & 7)) & 7) * 43) >> 3 : (((dtile + 2 * (r & 3)) & 7) * 43) >> 3;
  const int orot = MAPC ? 2 * ((r + 2 * (r >> 3)) & 7) : 2 * ((dtile + 2 * (r & 3)) & 7);
  auto c_src = [&](int n, rsrc_t& rw, u32& vo, u32& so, u32& st) {
    if (n >= 4) {
      int ks = 4 * (n - 4) + hw + crot; if (ks >= 43) ks -= 43;
      rw = mkrs(w2, F * S * 2); vo = (lane >> 3) * 4096 + cswz + stile * 128; so = ks * 131072; st = 32768;
    } else {
      rw = mkrs(wo, D * D * 2); vo = (lane >> 3) * 1024 + cswz + dtile * 128; so = ((4 * n + hw + orot) & 15) * 32768; st = 8192;
    }
  };
  auto c_issue = [&](int n) {
    rsrc_t rw; u32 vo, so, st;
    c_src(n, rw, vo, so, st);
    const u32 dst = ((6 + n) % 9) * 16384 + hw * 4096;
#pragma unroll
    for (int m = 0; m < 4; ++m) { if (n < NTW2) glds<true>(rw, vo, so + m * st, dst + m * 1024); else glds<false>(rw, vo, so + m * st, dst + m * 1024); }
  };
  const u32 aFULL = L_FULL + (lane & 3) * 4, aPROG = L_PROG + (lane & 3) * 4;
  int c_first = 0;
  u32 fullv = 0;
  auto pub = [&](u32 v) { if (v > fullv) { fullv = v; cw_st(L_FULL + hw * 4, v); } };

  if (roleA) {
    const int row0 = a_rb * 128, arot = r + 2 * (r >> 3);
    if (!isG) {
      const rsrc_t rw = mkrs(wqkv, D * 3 * D * 2);
      const u32 xvo = (row0 + 32 * hw + (lane >> 3)) * 1024, xsw = lane >> 4;
      const u32 wvo = (16 * hw + (lane >> 3)) * 3072 + cswz + a_grp * 128;
      const u32 xv0 = xvo + (((lane & 7) ^ xsw) << 4), xv1 = xvo + (((lane & 7) ^ (xsw + 4)) << 4);
      u32 dst = 0, cc = arot & 7;
      auto a_issue = [&]() {
#pragma unroll
        for (int i = 0; i < 4; ++i) glds<false>(rx, (i & 1) ? xv1 : xv0, i * 8192 + cc * 128, dst + hw * 4096 + i * 1024);
#pragma unroll
        for (int i = 0; i < 6; ++i) glds<false>(rw, wvo, (64 * cc + 8 * (i & 1)) * 3072 + (i >> 1) * 1024, dst + 16384 + (i >> 1) * 8192 + hw * 2048 + (i & 1) * 1024);
        dst = dst == 81920 ? 0 : dst + 40960; cc = (cc + 1) & 7;
      };
      a_issue();
      code_touch();
      ST(13);
      VMCNT(1);
      BARRIER();
      a_issue(); a_issue();
#pragma unroll 1
      for (int c = 1; c < 6; ++c) { VMCNT(10); BARRIER(); a_issue(); }
      VMCNT(10);
      BARRIER();
      if (w == 4) {
        flag_wait<1>(ws, WS_FU, xc, ID_HALL, 0, tag);
        const u32x4 stin = ld128c(rws, WS_STAT + row0 * 8 + lane * 16, 0);
        *(LAS u32x2*)(L_AST + lane * 8) = u32x2{stin.x, stin.z};
        *(LAS u32x2*)(L_AST + 512 + lane * 8) = u32x2{stin.y, stin.w};
      }
      VMCNT(0);
      BARRIER();
      if (PTOUCH) {
        const rsrc_t rw2 = mkrs(w2, F * S * 2);
#pragma unroll
        for (int q = 0; q < (MAPC ? 3 : 2); ++q) {
          u32 vo;
          if (!MAPC) { const int ln = (r * 3 + hw) * 64 + lane; vo = hw < 3 ? (ln >> 2) * 4096 + (xc + 4 * q) * 512 + (ln & 3) * 128 : ~0u; }
          else {
            const int pc = q * 4 + hw, ln = pc < 6 ? (r * 6 + pc) * 64 + lane : (((xc & 1) * 32 + r) * 3 + pc - 6) * 64 + lane;
            vo = pc < 9 ? (ln >> 3) * 4096 + ((xc >> 1) + (pc < 6 ? 0 : 2)) * 1024 + (ln & 7) * 128 : ~0u;
          }
          asm volatile("s_mov_b32 m0, %2\n\ts_nop 0\n\tbuffer_load_dword %0, %1, 0 offen lds" ::"v"(vo), "s"(rw2), "s"(L_DUMMY) : "memory");
        }
      }
      ST(2);
      BARRIER();
#pragma unroll
      for (int n = 0; n < APRE; ++n) c_issue(n);
      c_first = APRE;
      ST(3);
      wait_word(L_CNT + 4, 4);
      VMCNT(0);
      ST(7);
    } else {
      const int rb2 = w & 1, hp = w >> 1;
      u32x4 wA0, wA1, sel0, selA, selB;
      const u32x4 z4 = {0, 0, 0, 0};
      {
        const u16* p = wphi + (a_grp * 4 + 2 * hp + blk) * 256 + 4 * hh * 16 + l15;
        const u32x4 wq = {(u32)p[0] | ((u32)p[16] << 16), (u32)p[32] | ((u32)p[48] << 16), (u32)p[128] | ((u32)p[144] << 16), (u32)p[160] | ((u32)p[176] << 16)};
        wA0 = blk ? z4 : wq; wA1 = blk ? wq : z4;
        const int dlt = l15 - 4 * hh;
        const u32x4 sel = {dlt == 0 ? 0x3f80u : dlt == 1 ? 0x3f800000u : 0u, dlt == 2 ? 0x3f80u : dlt == 3 ? 0x3f800000u : 0u,
                           dlt == 8 ? 0x3f80u : dlt == 9 ? 0x3f800000u : 0u, dlt == 10 ? 0x3f80u : dlt == 11 ? 0x3f800000u : 0u};
        sel0 = blk ? z4 : sel;
        selA = blk == rb2 ? sel : z4; selB = blk == rb2 ? z4 : sel;
        PIN(wA0); PIN(wA1); PIN(sel0); PIN(selA); PIN(selB);
      }
      f32x16 aq[2] = {zero16, zero16}, ak[2] = {zero16, zero16}, av[2] = {zero16, zero16};
      f32x4 cs3[3] = {{0, 0, 0, 0}, {0, 0, 0, 0}, {0, 0, 0, 0}};
      const u32 one2 = 0x3f803f80u, csel1 = l15 == blk ? one2 : 0u;
      const u32x4 csel = {csel1, csel1, csel1, csel1};
      const u32 xb0 = (64 * rb2 + l31) * 128 + ((hh ^ ((l31 >> 1) & 7)) << 4);
      const u32 wb0 = 16384 + (8 * hh + (l15 >> 2)) * 128 + ((hp ^ (l15 >> 3)) << 6) + blk * 32 + (l15 & 3) * 8;
      u32x4 xa[2][2] = {{z4, z4}, {z4, z4}}, wa[2][3] = {{z4, z4, z4}, {z4, z4, z4}};
      u32 xb, wb, slot = 0;
      auto a_step = [&](int kc, bool rd, int kr) {
#pragma unroll
        for (int q = 0; q < 6; ++q) {
          const int sec = q >> 1, a = q & 1;
          if (rd) {
            if (q < 2) xa[kr & 1][q] = ldsr128((xb ^ (kr * 32)) + q * 4096);
            if (q >= 2 && q < 5) { const u32 ad = wb + (q - 2) * 8192 + kr * 2048; wa[kr & 1][q - 2] = cat(trd(ad), trd(ad + 512)); }
          }
          SB();
          if (sec == 0) { aq[a] = mfma(wa[kc & 1][0], xa[kc & 1][a], aq[a]); PIN(aq[a]); }
          if (sec == 1) { ak[a] = mfma(wa[kc & 1][1], xa[kc & 1][a], ak[a]); PIN(ak[a]); }
          if (sec == 2) { av[a] = mfma(xa[kc & 1][a], wa[kc & 1][2], av[a]); PIN(av[a]); }
          SB();
        }
        if (rb2 == (kc & 1)) {
#pragma unroll
          for (int q = 0; q < 3; ++q) { cs3[q] = mfma16(csel, wa[kc & 1][q], cs3[q]); PIN(cs3[q]); }
        }
      };
      ST(13);
#pragma unroll 1
      for (int c = 0; c < 8; ++c) {
        BARRIER();
        ST2R(16 + c);
        xb = opv(slot + xb0); wb = opv(slot + wb0);
        slot = slot == 81920 ? 0 : slot + 40960;
        a_step(3, true, 0); a_step(0, true, 1); a_step(1, true, 2); a_step(2, true, 3);
      }
      a_step(3, false, 0);
      if (lane < 16) {
        const u32 ca = L_ACS + hp * 384 + lane * 4;
#pragma unroll
        for (int q = 0; q < 3; ++q)
          asm volatile("ds_add_f32 %0, %1 offset:%3\n\tds_add_f32 %0, %2 offset:%4" ::"v"(ca), "v"(cs3[q][0]), "v"(cs3[q][1]), "i"(q * 128), "i"(q * 128 + 64) : "memory");
      }
      const u32 stb = opv(L_AST + (64 * rb2 + 4 * hh) * 4), stl = opv(L_AST + (64 * rb2 + l31) * 4);
      float sm[2], sr[2];
#pragma unroll
      for (int a = 0; a < 2; ++a) { sm[a] = *(LAS float*)(stl + a * 128); sr[a] = *(LAS float*)(stl + 512 + a * 128) * 1.4426950408889634f; }
      ST(2);
      BARRIER();
      auto feat = [&](const f32x16& acc, float m, const f32x4 (&c4)[4]) {
        u32 t[8];
        sfor<0, 8>([&](auto ic) { constexpr int k = decltype(ic)::v;
          t[k] = pk2v(pfma(sp2(-m), f32x2{c4[k >> 1][2 * (k & 1)], c4[k >> 1][2 * (k & 1) + 1]}, g2<k>(acc))); });
        return mfma(wA1, u32x4{t[4], t[5], t[6], t[7]}, mfma(wA0, u32x4{t[0], t[1], t[2], t[3]}, zero16));
      };
      const u32 csb = opv(L_ACS + hp * 384 + hh * 16);
      const u32 rec = (a_grp * 2 + hp) * 2 + rb2;
      {
        f32x4 ck[4];
#pragma unroll
        for (int i = 0; i < 4; ++i) ck[i] = ldsf4(csb + 128 + 32 * i);
        const float cv = *(LAS float*)(L_ACS + hp * 384 + 256 + l31 * 4);
        f32x16 pk[2];
#pragma unroll
        for (int a = 0; a < 2; ++a) pk[a] = feat(ak[a], sm[a], ck);
        f32x16 kv[2] = {zero16, zero16};
#pragma unroll
        for (int a = 0; a < 2; ++a) {
          f32x4 m4[4], r4[4];
#pragma unroll
          for (int i = 0; i < 4; ++i) { m4[i] = ldsf4(stb + a * 128 + 32 * i); r4[i] = ldsf4(stb + a * 128 + 512 + 32 * i); }
          u32x4 pos[2], neg[2];
          hedgehog(pk[a], sr[a], pos, neg);
          f32x16 t[2];
#pragma unroll
          for (int e = 0; e < 2; ++e) t[e] = mfma(neg[e], selB, mfma(pos[e], selA, zero16));
          u32 vb[8];
          sfor<0, 8>([&](auto ic) { constexpr int k = decltype(ic)::v;
            vb[k] = pk2v(pfma(f32x2{m4[k >> 1][2 * (k & 1)], m4[k >> 1][2 * (k & 1) + 1]}, sp2(-cv), g2<k>(av[a])) * f32x2{r4[k >> 1][2 * (k & 1)], r4[k >> 1][2 * (k & 1) + 1]}); });
#pragma unroll
          for (int e = 0; e < 2; ++e) {
            const bool own = blk == e;
            const u32x4 v0 = {own ? vb[0] : one2, own ? vb[1] : one2, own ? vb[2] : one2, own ? vb[3] : one2};
            const u32x4 v1 = {own ? vb[4] : one2, own ? vb[5] : one2, own ? vb[6] : one2, own ? vb[7] : one2};
            kv[e] = mfma(pk8<8>(t[e]), v1, mfma(pk8<0>(t[e]), v0, kv[e]));
          }
        }
        const u32 kab = L_AKV + w * 4352;
#pragma unroll
        for (int e = 0; e < 2; ++e) {
          if (blk == e || l15 == 0) {
            const u32 ka = opv(kab + (blk == e ? lane : 64 + e * 2 + hh) * 16);
            ldsw128(ka, get4<0>(kv[e])); ldsw128(ka + 1088, get4<4>(kv[e])); ldsw128(ka + 2176, get4<8>(kv[e])); ldsw128(ka + 3264, get4<12>(kv[e]));
          }
        }
        cw_st(L_SYN + w * 4, 1);
        const u32 zo = 1024 + ((l31 >> 2) & 1) * 1088 + (l31 >> 3) * 16 + (l31 & 3) * 4, kp = L_AKV + (w ^ 1) * 4352 + 2176;
        const f32x4 o0 = ldsf4(kab + lane * 16), o1 = ldsf4(kab + 1088 + lane * 16);
        const float oz = *(LAS float*)(kab + zo);
        ST(3);
        lds_wait<false>(L_SYN + (w ^ 1) * 4, 1);
        const f32x4 s0 = o0 + ldsf4(kp + lane * 16), s1 = o1 + ldsf4(kp + 1088 + lane * 16);
        const float zz = oz + *(LAS float*)(kp + zo);
        st128c(rws, WS_KV + (a_rb * 32 + rec) * 1024 + lane * 16, u32x4{pk2(s0[0], s0[1]), pk2(s0[2], s0[3]), pk2(s1[0], s1[1]), pk2(s1[2], s1[3])});
        if (lane < 32) __builtin_amdgcn_raw_buffer_store_b32(__float_as_uint(zz), rws, WS_Z + (a_rb * 32 + rec) * 128 + lane * 4, 0, 16);
      }
      u32x4 qp[2][2], qn[2][2];
      {
        f32x4 cq[4];
#pragma unroll
        for (int i = 0; i < 4; ++i) cq[i] = ldsf4(csb + 32 * i);
        const f32x16 p0 = feat(aq[0], sm[0], cq), p1 = feat(aq[1], sm[1], cq);
        publish(ws, WS_FKV, a_rb * 8 + a_grp, tag, L_CNT, 4, lane);
        ST(4);
        hedgehog(p0, sr[0], qp[0], qn[0]);
        hedgehog(p1, sr[1], qp[1], qn[1]);
#pragma unroll
        for (int a = 0; a < 2; ++a) { PIN(qp[a][0]); PIN(qp[a][1]); PIN(qn[a][0]); PIN(qn[a][1]); }
      }
      ST(5);
      if (w == 0) { flag_wait<1>(ws, WS_FKV, xc, l15 * 8 + a_grp, 0, tag); cw_st(L_LND, 1); }
      else lds_wait<false>(L_LND, 1);
      ST(6);
      u32x4 kmine;
      {
        u32x4 kvl[16]; u32 zl[16];
#pragma unroll
        for (int q = 0; q < 16; ++q) {
          kvl[q] = ld128c(rws, WS_KV + lane * 16, (q * 32 + rec) * 1024);
          zl[q] = __builtin_amdgcn_raw_buffer_load_b32(rws, WS_Z + l31 * 4, (q * 32 + rec) * 128, 16);
        }
        SB();
        f32x16 tot = zero16;
        float zz = 0.f;
#pragma unroll
        for (int q = 0; q < 16; ++q) { tot = mfma(sel0, kvl[q], tot); zz += __uint_as_float(zl[q]); }
        kmine = pk8<0>(tot);
        ldsw128(L_AT + w * 1024 + lane * 16, kmine);
        if (lane < 32) *(LAS u16*)(L_AZT + w * 64 + lane * 2) = (u16)pk2(zz, zz);
      }
      cw_st(L_SYN + w * 4, 2);
      lds_wait<false>(L_SYN + (w ^ 1) * 4, 2);
      ST(14);
      {
        const u32x4 k0 = ldsr128(L_AT + 2 * hp * 1024 + lane * 16), k1 = ldsr128(L_AT + (2 * hp + 1) * 1024 + lane * 16);
        const u32x4 y0 = ldsr128(L_AZT + 2 * hp * 64 + ((1 - blk) * 2 + hh) * 16), y1 = ldsr128(L_AZT + (2 * hp + 1) * 64 + ((1 - blk) * 2 + hh) * 16);
#pragma unroll
        for (int e = 0; e < 2; ++e) {
          const bool own = blk == e;
          const u32x4 a0 = own ? k0 : y0, a1 = own ? k1 : y1;
#pragma unroll
          for (int a = 0; a < 2; ++a) {
            const f32x16 nd = mfma(a1, qn[a][e], mfma(a0, qp[a][e], zero16));
            const float rd = __builtin_amdgcn_rcpf(nd[8 - 8 * e] + 1e-6f);
            const int o = 8 * e;
            st128c(rws, WS_O + ((a_grp * 4 + 2 * hp + e) * 64 + a_rb * 4 + 2 * rb2 + a) * 1024 + lane * 16,
                   u32x4{pk2(nd[o] * rd, nd[o + 1] * rd), pk2(nd[o + 2] * rd, nd[o + 3] * rd), pk2(nd[o + 4] * rd, nd[o + 5] * rd), pk2(nd[o + 6] * rd, nd[o + 7] * rd)});
          }
        }
        cw_st(L_PROG + w * 4, 64);
        publish(ws, WS_FO, a_rb * 8 + a_grp, tag, L_CNT + 4, 4, lane);
      }
      ST(7);
    }
  } else {
    const int rot = MAPB ? 8 * (b_dt + (r >> 3)) : 8 * b_dt + 4 * (r >> 2);
    if (isG) {
      const int li = (xc - 4) * 32 + r;
      const int rl = 4 * w + (lane >> 4), row = li * 16 + rl;
      u32x4 xr[4];
#pragma unroll
      for (int n = 0; n < 4; ++n) xr[n] = __builtin_amdgcn_raw_buffer_load_b128(rx, row * 1024 + l15 * 16 + n * 256, 0, 0);
      ST(13);
      float s1[2] = {0.f, 0.f}, s2[2] = {0.f, 0.f};
#pragma unroll
      for (int i = 0; i < 16; ++i) { const u32 v = xr[i >> 2][i & 3]; s1[i & 1] = dot2(v, 0x3f803f80u, s1[i & 1]); s2[i & 1] = dot2(v, v, s2[i & 1]); }
      ST(1);
      const float m = sum16(s1[0] + s1[1]) * (1.f / D);
      const float var = fmaxf(sum16(s2[0] + s2[1]) * (1.f / D) - m * m, 0.f);
      const float mb = bfr(m);
      const float ri = __builtin_amdgcn_rcpf(bfr(__builtin_amdgcn_sqrtf(bfr(bfr(var) + 1e-5f))));
      const float m2 = bfr((m - mb) * ri);
      const float r2 = __builtin_amdgcn_rcpf(bfr(__builtin_amdgcn_sqrtf(bfr(bfr(var * ri * ri) + 1e-5f))));
      const float ha = ri * r2, hb = -(mb * ri + m2) * r2;
      if (l15 == 0) *(LAS u32x2*)(L_LNS + rl * 8) = u32x2{__float_as_uint(mb), __float_as_uint(ri)};
      const u32 wa = opv(L_LN + rl * LN_PITCH + l15 * 16);
#pragma unroll
      for (int n = 0; n < 4; ++n) {
        u32x4 hv;
#pragma unroll
        for (int i = 0; i < 4; ++i) hv[i] = pk2v(pfma(unpk(xr[n][i]), sp2(ha), sp2(hb)));
        ldsw128(wa + n * 256, hv);
      }
      cw_st(L_SYN + w * 4, 1);
      ST(14);
      const u32 ad = opv(L_LN + (8 * hh + (l15 >> 2)) * LN_PITCH + w * 256 + blk * 32 + (l15 & 3) * 8);
      const u32 ho = WS_H + (li * 16 + 4 * w) * 1024 + lane * 16;
      lds_wait<false>(L_SYN + (lane & 3) * 4, 1);
#pragma unroll
      for (int rr = 0; rr < 4; ++rr) st128c(rws, ho + rr * 1024, cat(trd(ad + rr * 64), trd(ad + rr * 64 + 4 * LN_PITCH)));
      if (w == 0 && lane < 8) st128c(rws, WS_STAT + li * 128 + lane * 16, ldsr128(L_LNS + lane * 16));
      cw_st(L_SYN + w * 4, 2);
      ST(2);
      publish<1>(ws, WS_FH, li, tag, L_CNT + 12, 4, lane);
      ST(3);
    }
    if (isG) {
      if (roleB) {
        u32x2 bias[6];
#pragma unroll
        for (int t = 0; t < 6; ++t) { int f = b_ft * 96 + (t >> 1) * 32 + 16 * b_kz + 8 * (t & 1) + 4 * hh; if (f > F - 4) f = F - 4; bias[t] = *(const GAS u32x2*)(b1 + f); }
        f32x16 acc[3];
#pragma unroll
        for (int t = 0; t < 3; ++t) {
          acc[t] = zero16;
#pragma unroll
          for (int i = 0; i < 2; ++i) { acc[t][4 * i] = lo16(bias[2 * t + i].x); acc[t][4 * i + 1] = hi16(bias[2 * t + i].x); acc[t][4 * i + 2] = lo16(bias[2 * t + i].y); acc[t][4 * i + 3] = hi16(bias[2 * t + i].y); }
          PIN(acc[t]);
        }
        cw_st(L_PROG + w * 4, 32);
        if (w == 0) { flag_wait<1>(ws, WS_FU, xc, ID_HALL, 0, tag); cw_st(L_GO, 1); }
        else wait_word(L_GO, 1);
        ST(4);
        const u32 hv0 = WS_H + b_kz * (32 * 32768) + (b_dt * 4 + w) * 1024 + lane * 16, hv1 = hv0 + 16384;
        u32x4 hr[16];
        u32 hso = (rot & 31) * 32768;
        auto h_next = [&]() { hso = (hso + 32768) & (32 * 32768 - 1); };
#pragma unroll
        for (int i = 0; i < 8; ++i) { hr[2 * i] = ld128c(rws, hv0, hso); hr[2 * i + 1] = ld128c(rws, hv1, hso); h_next(); }
        SB();
        const u32 b0 = (8 * hh + (l15 >> 2)) * 192 + (blk ^ b_kz) * 32 + (l15 & 3) * 8;
        u32x4 wfr[2][6];
        u32 pos = 2, gb = opv(b0 + 2 * 24576);
        bool ready = true;
        auto b_rd = [&](int buf, int k, int t) {
          const u32 ad = gb + k * 6144 + (t / 3) * 3072 + (t % 3) * 64;
          wfr[buf][t] = cat(trd(ad), trd(ad + 768));
        };
        lds_wait<false>(aFULL, 9);
#pragma unroll
        for (int t = 0; t < 6; ++t) b_rd(0, 0, t);
        u32 progv = 32, need = 9;
        const u32 proga = L_PROG + w * 4;
        auto b_pair = [&](bool more) {
#pragma unroll
          for (int mi = 0; mi < 2; ++mi) {
            ST2R(7 + need);
            u32 fv, gbn;
#pragma unroll
            for (int k = 0; k < 4; ++k) {
              if (k == 3) {
                gb = gbn;
                if ((more || mi < 1) && __builtin_expect(!ready, 0)) lds_wait<false>(aFULL, need);
              }
#pragma unroll
              for (int t = 0; t < 6; ++t) {
                if (more || mi < 1 || k < 3) b_rd((k + 1) & 1, (k + 1) & 3, t);
                if (k == 2 && t == 1) fv = *(volatile LAS u32*)aFULL;
                if (k == 2 && t == 5) cw_st(proga, progv);
                SB();
                acc[t % 3] = mfma(wfr[k & 1][t], hr[(mi * 4 + k) * 2 + t / 3], acc[t % 3]);
                PIN(acc[t % 3]);
                SB();
                if (t == 2 && more) hr[(mi * 4 + k) * 2] = ld128c(rws, hv0, hso);
                if (t == 5 && more) { hr[(mi * 4 + k) * 2 + 1] = ld128c(rws, hv1, hso); h_next(); }
                if (k == 1 && t == 1) { progv += 4; PIN(progv); pos = pos == 5 ? 0 : pos + 1; ++need; }
                if (k == 1 && t == 3) gbn = opv(b0 + pos * 24576);
                if (k == 2 && t == 5) ready = __ballot(fv < need) == 0;
                SB();
              }
            }
          }
        };
#pragma unroll 1
        for (int mo = 0; mo < 3; ++mo) b_pair(true);
        b_pair(false);
        ST(5);
        const u32 pa = WS_P + w * 6144 + lane * 16;
#pragma unroll
        for (int t = 0; t < 3; ++t) {
          st128c(rws, pa + b_id * 24576 + 2 * t * 1024, get4<8>(acc[t]));
          st128c(rws, pa + b_id * 24576 + (2 * t + 1) * 1024, get4<12>(acc[t]));
        }
        publish<1>(ws, WS_FP, b_id, tag, L_CNT + 4, 4, lane);
        ST(30);
        if (w == 0) { flag_wait<1>(ws, WS_FP, 0, b_id ^ 1, 0, tag); cw_st(L_LND, 1); }
        else lds_wait<false>(L_LND, 1);
        ST(31);
        u32x4 oth[6];
#pragma unroll
        for (int t = 0; t < 6; ++t) oth[t] = ld128c(rws, pa + (b_id ^ 1) * 24576 + t * 1024, 0);
        SB();
#pragma unroll
        for (int t = 0; t < 3; ++t) {
          u32x4 uv;
#pragma unroll
          for (int i = 0; i < 4; ++i) {
            const f32x2 v = f32x2{acc[t][2 * i], acc[t][2 * i + 1]} + f32x2{__uint_as_float(oth[2 * t + (i >> 1)][2 * (i & 1)]), __uint_as_float(oth[2 * t + (i >> 1)][2 * (i & 1) + 1])};
            const float c0 = -2.f * 0.7978845608028654f * 1.4426950408889634f;
            const f32x2 zz = v * pfma(v * v, sp2(0.044715f * c0), sp2(c0));
            uv[i] = pk2v(v * rc2(ex2(zz) + sp2(1.f)));
          }
          if (b_ft * 6 + 2 * t < 86) st128c(rws, WS_U + ((b_ft * 6 + 2 * t + b_kz) * 16 + b_dt * 4 + w) * 1024 + lane * 16, uv);
        }
        ST(6);
        publish(ws, WS_FU, b_id, tag, L_CNT, 4, lane);
        ST(7);
      } else {
        if (bid == 8 * 31 + 7 && w == 0) {
          flag_wait<2>(ws, WS_FH, 0, lane, lane + 64, tag);
          flag_set(ws, WS_FU, ID_HALL, tag, lane);
        }
        cw_st(L_PROG + w * 4, 64);
      }
    } else if (roleB) {
      const rsrc_t rw1 = mkrs(w1, S * F * 2);
      u32 vo[6];
      {
        u32 rr = (lane * 43) >> 9, ch = lane - rr * 12;
        const u32 cm = b_ft == 14 ? 3 : 15;
#pragma unroll
        for (int n = 0; n < 6; ++n) {
          vo[n] = rr * (F * 2) + (ch & cm) * 16 + b_ft * 192;
          ch += 4; rr += 5;
          if (ch >= 12) { ch -= 12; ++rr; }
        }
      }
      u32 pos = 2;
      auto b_issue = [&](int m) {
        const u32 so = (b_kz * 32 + ((4 * m + hw + rot) & 31)) * (32 * F * 2);
#pragma unroll
        for (int n = 0; n < 6; ++n) glds<false>(rw1, vo[n], so, pos * 24576 + hw * 6144 + n * 1024);
      };
      b_issue(8);
      ST(13);
      lds_wait<true>(L_SYN + (lane & 3) * 4, 2);
      code_touch();
      ST(16);
#pragma unroll 1
      for (int m = 9; m < 16; ++m) {
        pos = pos == 5 ? 0 : pos + 1;
        if (m >= 14) { VMCNT(0); pub(m); lds_wait<true>(aPROG, 4 * (m - 5)); }
        b_issue(m);
        VMCNT(6 * BDEP);
        pub(m + 1 - BDEP);
        if (m == 13) ST(17);
      }
      ST(18);
      VMCNT(12); pub(14); VMCNT(6); pub(15); VMCNT(0); pub(16);
      ST(4);
    }
  }

  if (isG) {
    const int p = w & 1, kh = w >> 1;
    const int srow = stile * 64 + kh * 32 + l31;
    u32x2 xin[4];
#pragma unroll
    for (int t = 0; t < 4; ++t) xin[t] = *(const GAS u32x2*)(x + srow * D + dtile * 64 + p * 32 + 8 * t + 4 * hh);
    const u32 b2v = b2[srow];
    if (w == 0) {
      flag_wait<1>(ws, hh ? WS_FO : WS_FU, xc, hh ? (stile >> 1) * 8 + (lane & 7) : ((l31 < 30 ? l31 >> 1 : 0) * 4 + (dtile >> 1)) * 2 + (lane & 1), 0, tag);
      cw_st(L_GOO, 1);
    } else wait_word(L_GOO, 1);
    ST(8);
    const u32 uv0 = WS_U + (dtile * 2 + p) * 1024 + lane * 16, uv1 = uv0 + 16384, ov0 = WS_O + (stile * 2 + p) * 1024 + lane * 16, ov1 = ov0 + 65536;
    u32x4 ar[16], orr[16];
    u32 uso = (kh + crot) * 32768, oso = ((kh + orot) & 15) * 131072;
    auto a_load = [&](int i, int h) {
      if (i >= 8) {
        ar[(2 * i + h) & 15] = ld128c(rws, h ? uv1 : uv0, uso);
        if (h) { uso += 65536; if (uso >= 43 * 32768) uso -= 43 * 32768; }
      } else {
        orr[2 * i + h] = ld128c(rws, h ? ov1 : ov0, oso);
        if (h) oso = (oso + 262144) & (16 * 131072 - 1);
      }
    };
#pragma unroll
    for (int i = 0; i < 8; ++i) { a_load(i, 0); a_load(i, 1); }
    SB();
    f32x16 ac[2] = {zero16, zero16};
    u32 tb[2], tg[2];
#pragma unroll
    for (int t = 0; t < 2; ++t) tb[t] = (4 * hh + (l15 >> 2)) * 128 + ((t ^ kh ^ (l15 >> 3)) << 6) + blk * 32 + (l15 & 3) * 8 + kh * 4096;
    u32x4 wfr[2][4];
    auto c_rd = [&](auto ic, int t) {
      constexpr int i = decltype(ic)::v;
      if (i >= 30) return;
      const u32 ad = tg[t & 1] + (i & 1) * 8192 + (t >> 1) * 2048;
      wfr[i & 1][t] = cat(trd(ad), trd(ad + 1024));
    };
    lds_wait<false>(aFULL, 17);
    tg[0] = opv(tb[0] + 6 * 16384); tg[1] = opv(tb[1] + 6 * 16384);
#pragma unroll
    for (int t = 0; t < 4; ++t) c_rd(IC<0>{}, t);
    u32 fv, tn[2], progv = 64;
    u32x4 skeep[2];
    bool ready = true;
    f32x2 sv[8];
    const u32 proga = L_PROG + w * 4;
    sfor<0, 30>([&](auto ic) {
      constexpr int i = decltype(ic)::v;
      constexpr bool ld = i + 8 < 30;
      if ((i & 1) == 0) ST2(32 + i / 2);
      if (i == 8) {
        ST(9);
        const u32x4 a0 = pk8<0>(ac[0]), a1 = pk8<8>(ac[0]), b0 = pk8<0>(ac[1]), b1 = pk8<8>(ac[1]);
        const u32 sa = opv(L_CS + w * 2048 + lane * 16), sb = opv(L_CS + (4 + p) * 2048 + lane * 16);
        ldsw128(sa, b0); ldsw128(sa + 1024, b1);
        if (kh != p) { ldsw128(sb, a0); ldsw128(sb + 1024, a1); }
        skeep[0] = a0; skeep[1] = a1;
        cw_st(L_SYN + w * 4, 6);
        ac[1] = zero16;
#pragma unroll
        for (int q = 0; q < 16; ++q) ac[0][q] = lo16(b2v);
        ST(15);
      }
      if (i == 14) {
        lds_wait<false>(L_SYN + (lane & 3) * 4, 6);
        const u32 s1 = L_CS + (kh + 2 * (1 - p)) * 2048 + lane * 16, s2 = L_CS + (4 + kh) * 2048 + lane * 16;
        const u32x4 c0 = ldsr128(s1), c1 = ldsr128(s1 + 1024);
        u32x4 d0 = skeep[0], d1 = skeep[1];
        if (kh != p) { d0 = ldsr128(s2); d1 = ldsr128(s2 + 1024); }
#pragma unroll
        for (int q = 0; q < 4; ++q) { sv[q] = unpk(c0[q]) + unpk(d0[q]); sv[4 + q] = unpk(c1[q]) + unpk(d1[q]); PIN(sv[q]); PIN(sv[4 + q]); }
      }
      if (i == 29 && kh == 1) { ar[10] = u32x4{0, 0, 0, 0}; ar[11] = ar[10]; }
      if ((i & 1) && i < 29) {
        tg[0] = tn[0]; tg[1] = tn[1];
        if (__builtin_expect(!ready, 0)) lds_wait<false>(aFULL, 16 + i / 2 + 2);
      }
#pragma unroll
      for (int t = 0; t < 4; ++t) {
        c_rd(IC<i + 1>{}, t);
        if (!(i & 1) && t == 0) fv = *(volatile LAS u32*)(i < 28 ? aFULL : aPROG);
        if (!(i & 1) && t == 3) cw_st(proga, progv);
        SB();
        if (i >= 8) ac[t & 1] = mfma(ar[(2 * i + (t >> 1)) & 15], wfr[i & 1][t], ac[t & 1]);
        else ac[t & 1] = mfma(wfr[i & 1][t], orr[(2 * i + (t >> 1)) & 15], ac[t & 1]);
        PIN(ac[t & 1]);
        SB();
        if (ld && (t & 1)) a_load(i + 8, t >> 1);
        if (!(i & 1) && t == 0) { progv += 4; PIN(progv); tn[0] = opv(tb[0] + ((7 + i / 2) % 9) * 16384); tn[1] = opv(tb[1] + ((7 + i / 2) % 9) * 16384); }
        if (!(i & 1) && t == 3) ready = __ballot(fv < (i < 28 ? 16 + i / 2 + 2 : 64 + 36)) == 0;
        SB();
      }
    });
    ST(10);
    if (!ready) lds_wait<false>(aPROG, 64 + 36);
    const u32 ya = opv(L_CY + w * 4096 + lane * 16);
    ldsw128(ya, get4<0>(ac[1])); ldsw128(ya + 1024, get4<4>(ac[1])); ldsw128(ya + 2048, get4<8>(ac[1])); ldsw128(ya + 3072, get4<12>(ac[1]));
    cw_st(L_SYN + w * 4, 7);
    lds_wait<false>(L_SYN + (w ^ 2) * 4, 7);
    f32x4 yo[4];
#pragma unroll
    for (int t = 0; t < 4; ++t) yo[t] = ldsf4((ya ^ 8192) + t * 1024);
    SB();
#pragma unroll
    for (int t = 0; t < 4; ++t) {
      u32x2 ov;
#pragma unroll
      for (int i = 0; i < 2; ++i) {
        const f32x2 yy = f32x2{ac[0][4 * t + 2 * i], ac[0][4 * t + 2 * i + 1]} + f32x2{yo[t][2 * i], yo[t][2 * i + 1]};
        const f32x2 gg = yy * rc2(ex2(yy * sp2(-1.4426950408889634f)) + sp2(1.f));
        ov[i] = pk2v(pfma(sv[2 * t + i], gg, unpk(xin[t][i])));
      }
      *(GAS u32x2*)(out + srow * D + dtile * 64 + p * 32 + 8 * t + 4 * hh) = ov;
    }
  } else {
    u32x4 rc[6][4];
    lds_wait<true>(aPROG, CHOLD);
#pragma unroll
    for (int n = 0; n < 9; ++n) {
      if (n >= c_first) {
        if (n > 2 && n < 8) lds_wait<true>(aPROG, c_need(n));
        if (n == HOLDN && roleB) wait_word(L_CNT + 4, 4);
        c_issue(n);
      }
      if (n == 5 && !CSTAGE) { pub(16 + c_first); VMCNT(8); pub(16 + 4); }
      if (n == 5 && CSTAGE) {
        pub(16 + c_first);
#pragma unroll
        for (int q = 9; q < 15; ++q) {
          rsrc_t rw; u32 vo2, so2, st2;
          c_src(q, rw, vo2, so2, st2);
#pragma unroll
          for (int m = 0; m < 4; ++m) rc[q - 9][m] = __builtin_amdgcn_raw_buffer_load_b128(rw, vo2, so2 + m * st2, 0);
          if (q == 10) { SB(); VMCNT(16); pub(16 + 4); }
        }
        SB();
      }
      ST2(32 + n);
    }
    if (CSTAGE) VMCNT(36); else VMCNT(12);
    pub(16 + 6);
    VMCNT(0); pub(16 + 9);
    ST(8);
    ST(9);
    const u32 wa = hw * 4096 + lane * 16;
#pragma unroll
    for (int n = 9; n < 15; ++n) {
      lds_wait<true>(aPROG, c_need(n));
      if (CSTAGE) {
        const u32 wb = opv(wa + ((6 + n) % 9) * 16384);
#pragma unroll
        for (int m = 0; m < 4; ++m) ldsw128(wb + m * 1024, rc[n - 9][m]);
        pub(16 + n + 1);
      } else {
        c_issue(n);
        if (n >= 12) { VMCNT(8); pub(16 + n - 1); }
        if (n == 14) { VMCNT(4); pub(30); VMCNT(0); pub(31); }
      }
      ST2(32 + n);
    }
    if (w == 4 && bid == 0) {
      if (lane == 0) __hip_atomic_store((GAS u64*)(ws + WS_EPOCH), tag, __ATOMIC_RELAXED, __HIP_MEMORY_SCOPE_AGENT);
    }
  }
#endif
}
#undef tag
}

extern "C" void kernel_launch(void* const* d_in, const int* in_sizes, int n_in, void* d_out, int out_size, void* d_ws, size_t ws_size,
                              hipStream_t stream) {
  using namespace hk;
  (void)hipFuncSetAttribute((const void*)fused, hipFuncAttributeMaxDynamicSharedMemorySize, LDS_BYTES);
  fused<<<256, 512, LDS_BYTES, stream>>>((const u16*)d_in[0], (const u16*)d_in[1], (const u16*)d_in[2], (const u16*)d_in[3], (const u16*)d_in[5],
                                         (const u16*)d_in[6], (const u16*)d_in[7], (const u16*)d_in[8], (u16*)d_out, (char*)d_ws);
}
```

```cpp
#define RSWAP 1
#include <hip/hip_runtime.h>
#include <stdint.h>

namespace hk {
typedef unsigned short u16;
typedef unsigned u32;
typedef unsigned long long u64;
typedef u32 u32x4 __attribute__((ext_vector_type(4)));
typedef u32 u32x2 __attribute__((ext_vector_type(2)));
typedef float f32x4 __attribute__((ext_vector_type(4)));
typedef float f32x2 __attribute__((ext_vector_type(2)));
typedef float f32x16 __attribute__((ext_vector_type(16)));
typedef __bf16 bf16x8 __attribute__((ext_vector_type(8)));
typedef __bf16 bf16x2 __attribute__((ext_vector_type(2)));
typedef short s16x4 __attribute__((ext_vector_type(4)));
#define LAS __attribute__((address_space(3)))
#define GAS __attribute__((address_space(1)))
#define DI __device__ __forceinline__
typedef __amdgpu_buffer_rsrc_t rsrc_t;

#define NT 0
#define PAUSE 0
#define APRE2 APRE
#define APRIO 0
#define DELAYA 0
#define DELAYB 0
#define CSTAGE 1
#define CHOLD 48
#define HOLDN 6
#define HDRAIN 1
#define PTAG 0
#define PDELAY 16
#define BPRE 1
#define EARLY 0
#define OWAIT 1
#define HOLDGO 99
#define HOLDU 0
#define AFIRST 1
#define PTOUCH 1
#define PSLEEP 1
#define NTW 0
#define NTX 2
#define NTW1 0
#define NTW2 0
#define BDEP 3
#define APRE 4
#define MAPB 0
#define HPRE 6
#define MAPC 1
constexpr int S = 2048, D = 512, F = 1376;
constexpr u32 WS_EPOCH = 0;
#define FSTR 128
constexpr u32 FL_SZ = 8 * 128 * 128;
constexpr u32 WS_FH = 4096, WS_FKV = WS_FH + FL_SZ, WS_FO = WS_FKV + FL_SZ, WS_FU = WS_FO + FL_SZ, WS_FP = WS_FH + 128 * 128;
constexpr int ID_HALL = 127;
constexpr u32 WS_STAT = WS_FU + FL_SZ;
constexpr u32 WS_Z = WS_STAT + S * 8;
constexpr u32 WS_KV = WS_Z + 16 * 32 * 128;
constexpr u32 WS_H = WS_KV + 16 * 32 * 1024;
constexpr u32 WS_O = WS_H + S * D * 2;
constexpr u32 WS_U = WS_O + S * D * 2;
constexpr u32 WS_P = WS_U + D * F * 2;
constexpr u32 WS_END = WS_P + 120 * 24576;
constexpr u32 WS_STAMP = 64u << 20;
constexpr u32 RING = 147456, L_SCR = RING, L_CTL = 163840 - 256, LDS_BYTES = 163840;
constexpr u32 L_FULL = L_CTL, L_PROG = L_CTL + 16, L_GO = L_CTL + 32, L_GOO = L_CTL + 36, L_LND = L_CTL + 40, L_CNT = L_CTL + 48, L_SYN = L_CTL + 96;
constexpr u32 L_LN = 122880, LN_PITCH = 1088, L_LNS = L_LN + 16 * LN_PITCH, L_DUMMY = L_CTL - 256;
constexpr u32 L_ACS = L_CTL - 1024, L_AST = L_SCR + 8192;
constexpr u32 L_AKV = 49152, L_AT = L_AKV + 8 * 4352, L_AZT = L_AT + 4096;
constexpr u32 L_BX = 49152, L_CY = 65536, L_CS = L_SCR;
constexpr u32 CODE_BYTES = 36 * 1024;
constexpr u32 SPIN_G = 1u << 15, SPIN_L = 1u << 20;

DI f32x16 mfma(u32x4 a, u32x4 b, f32x16 c) {
  return __builtin_amdgcn_mfma_f32_32x32x16_bf16(__builtin_bit_cast(bf16x8, a), __builtin_bit_cast(bf16x8, b), c, 0, 0, 0);
}
DI u32 pk2(float a, float b) { f32x2 v = {a, b}; return __builtin_bit_cast(u32, __builtin_convertvector(v, bf16x2)); }
DI float lo16(u32 p) { return __uint_as_float(p << 16); }
DI float hi16(u32 p) { return __uint_as_float(p & 0xffff0000u); }
DI float bfr(float a) { return lo16(pk2(a, a)); }
template <int O> DI u32x4 pk8(const f32x16& v) { return u32x4{pk2(v[O], v[O + 1]), pk2(v[O + 2], v[O + 3]), pk2(v[O + 4], v[O + 5]), pk2(v[O + 6], v[O + 7])}; }
template <int O> DI u32x4 get4(const f32x16& v) { return __builtin_bit_cast(u32x4, f32x4{v[O], v[O + 1], v[O + 2], v[O + 3]}); }
DI u32x2 trd(u32 addr) { return __builtin_bit_cast(u32x2, __builtin_amdgcn_ds_read_tr16_b64_v4i16((LAS s16x4*)addr)); }
DI u32x4 cat(u32x2 a, u32x2 b) { return u32x4{a.x, a.y, b.x, b.y}; }
DI u32x4 ldsr128(u32 addr) { return *(LAS u32x4*)addr; }
DI f32x4 ldsf4(u32 addr) { return *(LAS f32x4*)addr; }
DI void ldsw128(u32 addr, u32x4 v) { *(LAS u32x4*)addr = v; }
DI void lds_addf(u32 addr, float v) { (void)__hip_atomic_fetch_add((LAS float*)addr, v, __ATOMIC_RELAXED, __HIP_MEMORY_SCOPE_WORKGROUP); }
DI rsrc_t mkrs(const void* p, u32 bytes) { return __builtin_amdgcn_make_buffer_rsrc((void*)p, 0, bytes, 0x00020000); }
DI u32x4 ld128c(rsrc_t r, u32 vo, u32 so) { return __builtin_amdgcn_raw_buffer_load_b128(r, vo, so, 16); }
DI void st128c(rsrc_t r, u32 vo, u32x4 v) { __builtin_amdgcn_raw_buffer_store_b128(v, r, vo, 0, 16); }
template <bool STREAM> DI void glds(rsrc_t r, u32 vo, u32 so, u32 lds) {
  if (STREAM && NT) asm volatile("s_mov_b32 m0, %3\n\ts_nop 0\n\tbuffer_load_dwordx4 %0, %1, %2 offen nt lds" ::"v"(vo), "s"(r), "s"(so), "s"(lds) : "memory");
  else asm volatile("s_mov_b32 m0, %3\n\ts_nop 0\n\tbuffer_load_dwordx4 %0, %1, %2 offen lds" ::"v"(vo), "s"(r), "s"(so), "s"(lds) : "memory");
}
#define VMCNT(n) asm volatile("s_waitcnt vmcnt(%0)" ::"i"(n) : "memory")
#define BARRIER() asm volatile("s_waitcnt lgkmcnt(0)\n\ts_barrier" ::: "memory")
#define SB() __builtin_amdgcn_sched_barrier(0)
#define PIN(x) asm volatile("" : "+v"(x))
DI u32 opv(u32 v) { PIN(v); return v; }
template <int CTRL> DI float dppf(float v) { return __uint_as_float(__builtin_amdgcn_update_dpp(0u, __float_as_uint(v), CTRL, 0xf, 0xf, true)); }
DI float fmax2(float a, float b) { return __builtin_elementwise_maximum(a, b); }
DI float fmin2(float a, float b) { return __builtin_elementwise_minimum(a, b); }
DI f32x4 mfma16(u32x4 a, u32x4 b, f32x4 c) {
  return __builtin_amdgcn_mfma_f32_16x16x32_bf16(__builtin_bit_cast(bf16x8, a), __builtin_bit_cast(bf16x8, b), c, 0, 0, 0);
}
DI f32x2 pfma(f32x2 a, f32x2 b, f32x2 c) { return __builtin_elementwise_fma(a, b, c); }
DI f32x2 sp2(float a) { return f32x2{a, a}; }
DI f32x2 ex2(f32x2 a) { return f32x2{__builtin_amdgcn_exp2f(a.x), __builtin_amdgcn_exp2f(a.y)}; }
DI f32x2 rc2(f32x2 a) { return f32x2{__builtin_amdgcn_rcpf(a.x), __builtin_amdgcn_rcpf(a.y)}; }
DI u32 pk2v(f32x2 v) { return __builtin_bit_cast(u32, __builtin_convertvector(v, bf16x2)); }
DI f32x2 unpk(u32 p) { return f32x2{lo16(p), hi16(p)}; }
template <int I> DI f32x2 g2(const f32x16& v) { return f32x2{v[2 * I], v[2 * I + 1]}; }
DI void swap16(float& a, float& b) { auto r = __builtin_amdgcn_permlane16_swap(__float_as_uint(a), __float_as_uint(b), false, false); a = __uint_as_float(r[0]); b = __uint_as_float(r[1]); }
DI void swap32(float& a, float& b) { auto r = __builtin_amdgcn_permlane32_swap(__float_as_uint(a), __float_as_uint(b), false, false); a = __uint_as_float(r[0]); b = __uint_as_float(r[1]); }
DI float sum16(float v) { v += dppf<0xB1>(v); v += dppf<0x4E>(v); v += dppf<0x141>(v); v += dppf<0x140>(v); return v; }
template <bool MAX> DI void xred2(float& a, float& b) {
  swap32(a, b);
  a = MAX ? fmax2(a, b) : a + b;
  b = a; swap32(a, b);
}
DI float dot2(u32 a, u32 b, float c) { return __builtin_amdgcn_fdot2_f32_bf16(__builtin_bit_cast(bf16x2, a), __builtin_bit_cast(bf16x2, b), c, false); }
template <int V> struct IC { static constexpr int v = V; };
template <int I, int N, class Fn> DI void sfor(Fn&& f) { if constexpr (I < N) { f(IC<I>{}); sfor<I + 1, N>(f); } }
DI void cw_st(u32 addr, u32 val) { asm volatile("ds_write_b32 %0, %1" ::"v"(addr), "v"(val) : "memory"); }
template <bool SLEEP> DI void lds_wait(u32 addr, u32 need) {
  u32 v, cnt = SPIN_L;
  if (SLEEP)
    asm volatile("s_branch 2f\n1:\n\ts_sleep 2\n2:\n\tds_read_b32 %0, %2\n\ts_waitcnt lgkmcnt(0)\n\tv_cmp_gt_u32 vcc, %3, %0\n\ts_sub_u32 %1, %1, 1\n\t"
                 "s_cselect_b64 vcc, 0, vcc\n\ts_cmp_lg_u64 vcc, 0\n\ts_cbranch_scc1 1b" : "=&v"(v), "+s"(cnt) : "v"(addr), "s"(need) : "vcc", "scc", "memory");
  else
    asm volatile("1:\n\tds_read_b32 %0, %2\n\ts_waitcnt lgkmcnt(0)\n\tv_cmp_gt_u32 vcc, %3, %0\n\ts_sub_u32 %1, %1, 1\n\t"
                 "s_cselect_b64 vcc, 0, vcc\n\ts_cmp_lg_u64 vcc, 0\n\ts_cbranch_scc1 1b" : "=&v"(v), "+s"(cnt) : "v"(addr), "s"(need) : "vcc", "scc", "memory");
}
DI bool lds_test(u32 addr, u32 need) {
  u32 v; asm volatile("ds_read_b32 %0, %1\n\ts_waitcnt lgkmcnt(0)" : "=v"(v) : "v"(addr) : "memory");
  return __ballot(v < need) == 0;
}
DI void wait_word(u32 addr, u32 need) { lds_wait<true>(addr, need); }
DI u32 cw_add(u32 addr, int lane) {
  u32 old = 0;
  if (lane == 0) asm volatile("ds_add_rtn_u32 %0, %1, %2\n\ts_waitcnt lgkmcnt(0)" : "=v"(old) : "v"(addr), "v"(1u) : "memory");
  return __builtin_amdgcn_readfirstlane(old);
}
template <int NREP = 8> DI void flag_set(char* ws, u32 base, int id, u64 tg, int lane) {
  if (lane < NREP) __hip_atomic_store((GAS u64*)(ws + base + (lane * 128 + id) * FSTR), tg, __ATOMIC_RELAXED, __HIP_MEMORY_SCOPE_AGENT);
}
template <int N> DI void flag_wait(char* ws, u32 base, int rep, int id0, int id1, u64 tg) {
  GAS u64* p0 = (GAS u64*)(ws + base + (rep * 128 + id0) * FSTR);
  GAS u64* p1 = (GAS u64*)(ws + base + (rep * 128 + id1) * FSTR);
#pragma unroll 1
  for (u32 sp = 0; sp < SPIN_G; ++sp) {
    bool ok = __hip_atomic_load(p0, __ATOMIC_RELAXED, __HIP_MEMORY_SCOPE_AGENT) == tg;
    if (N > 1) ok &= __hip_atomic_load(p1, __ATOMIC_RELAXED, __HIP_MEMORY_SCOPE_AGENT) == tg;
    if (__all(ok)) break;
    __builtin_amdgcn_s_sleep(PSLEEP);
  }
  __builtin_amdgcn_fence(__ATOMIC_ACQUIRE, "wavefront");
  asm volatile("" ::: "memory");
}
template <int NREP = 8> DI void publish(char* ws, u32 base, int id, u64 tg, u32 cnt, u32 nw, int lane) {
  VMCNT(0);
  if (cw_add(cnt, lane) == nw - 1) flag_set<NREP>(ws, base, id, tg, lane);
}

#define ST(i)
#define ST2(i)
#define ST2R(i)

DI void hedgehog(const f32x16& p, float sc, u32x4 (&pos)[2], u32x4 (&neg)[2]) {
  float mx[2], mn[2];
#pragma unroll
  for (int e = 0; e < 2; ++e) {
    const int o = 8 * e;
    mx[e] = fmax2(fmax2(fmax2(fmax2(p[o], p[o + 1]), p[o + 2]), fmax2(fmax2(p[o + 3], p[o + 4]), p[o + 5])), fmax2(p[o + 6], p[o + 7]));
    mn[e] = -fmin2(fmin2(fmin2(fmin2(p[o], p[o + 1]), p[o + 2]), fmin2(fmin2(p[o + 3], p[o + 4]), p[o + 5])), fmin2(p[o + 6], p[o + 7]));
  }
  xred2<true>(mx[0], mn[0]); xred2<true>(mx[1], mn[1]);
  f32x2 ep[8], en[8];
  sfor<0, 8>([&](auto ic) {
    constexpr int k = decltype(ic)::v;
    ep[k] = pfma(g2<k>(p), sp2(sc), sp2(-mx[k >> 2] * sc));
    en[k] = pfma(g2<k>(p), sp2(-sc), sp2(-mn[k >> 2] * sc));
  });
#pragma unroll
  for (int k = 0; k < 8; ++k) { ep[k] = ex2(ep[k]); en[k] = ex2(en[k]); }
#pragma unroll
  for (int e = 0; e < 2; ++e) {
    const int o = 4 * e;
    const f32x2 a = (ep[o] + ep[o + 1]) + (ep[o + 2] + ep[o + 3]), b = (en[o] + en[o + 1]) + (en[o + 2] + en[o + 3]);
    float sp = a.x + a.y, sn = b.x + b.y;
    xred2<false>(sp, sn);
    const f32x2 rs = sp2(__builtin_amdgcn_rcpf(sp)), rn = sp2(__builtin_amdgcn_rcpf(sn));
#pragma unroll
    for (int k = 0; k < 4; ++k) { pos[e][k] = pk2v(ep[o + k] * rs); neg[e][k] = pk2v(en[o + k] * rn); }
  }
}

constexpr int c_need(int n) { return n < 9 ? (n <= 2 ? 48 : n == 3 ? 52 : n <= 5 ? 56 : n == 6 ? 60 : 64) : 64 + 4 * (n - 8); }

__global__ __launch_bounds__(512, 2) void fused(const u16* __restrict__ x, const u16* __restrict__ wqkv, const u16* __restrict__ wphi,
                                                const u16* __restrict__ wo, const u16* __restrict__ w1, const u16* __restrict__ b1,
                                                const u16* __restrict__ w2, const u16* __restrict__ b2, u16* __restrict__ out, char* ws) {
#if __HIP_DEVICE_COMPILE__
  ST(0);
  u64 pc0; asm volatile("s_getpc_b64 %0" : "=s"(pc0));
  const int tid = threadIdx.x, lane = tid & 63, w = __builtin_amdgcn_readfirstlane(tid >> 6);
  const int l15 = lane & 15, l31 = lane & 31, hh = lane >> 5, blk = (lane >> 4) & 1;
  const int bid = blockIdx.x ^ (RSWAP ? 4 : 0), xc = bid & 7, r = bid >> 3;
  const bool isG = w < 4;
  const int hw = w & 3;
  const int stile = MAPC ? 8 * (xc >> 1) + (r & 7) : 4 * xc + (r & 3), dtile = MAPC ? 4 * (xc & 1) + (r >> 3) : r >> 2;
  const bool roleA = xc < 4;
  const int b_dt = r & 3, b_kz = MAPB ? (r >> 2) & 1 : xc & 1, b_ft = MAPB ? 4 * (xc - 4) + (r >> 3) : 8 * ((xc >> 1) & 1) + (r >> 2);
  const int b_id = (b_ft * 4 + b_dt) * 2 + b_kz;
  const bool roleB = !roleA && b_ft < 15;
  const int a_rb = 8 * (xc >> 1) + (r & 7), a_grp = 4 * (xc & 1) + (r >> 3);
  const rsrc_t rws = mkrs(ws, WS_END);
  const rsrc_t rx = mkrs(x, S * D * 2);
  const f32x16 zero16 = {0, 0, 0, 0, 0, 0, 0, 0, 0, 0, 0, 0, 0, 0, 0, 0};

  const u64 epoch = __hip_atomic_load((GAS u64*)(ws + WS_EPOCH), __ATOMIC_RELAXED, __HIP_MEMORY_SCOPE_AGENT);
#define tag (epoch + 1)
  if (tid < 320) *(LAS u32*)(L_CTL - 1024 + tid * 4) = 0;
  if (!EARLY) BARRIER();
  ST(12);
  auto code_touch = [&]() {
    const rsrc_t rc = mkrs((const char*)(pc0 & ~255ull) + r * (CODE_BYTES / 32), w == 7 ? CODE_BYTES / 32 : 0);
    asm volatile("s_mov_b32 m0, %2\n\ts_nop 0\n\tbuffer_load_dword %0, %1, 0 offen lds" ::"v"(lane * 128), "s"(rc), "s"(L_DUMMY) : "memory");
  };
  const u32 cswz = ((lane & 7) ^ (blk << 2)) << 4;
  const int crot = MAPC ? ((((r >> 3) * 2 + (r & 7)) & 7) * 43) >> 3 : (((dtile + 2 * (r & 3)) & 7) * 43) >> 3;
  const int orot = MAPC ? 2 * ((r + 2 * (r >> 3)) & 7) : 2 * ((dtile + 2 * (r & 3)) & 7);
  auto c_src = [&](int n, rsrc_t& rw, u32& vo, u32& so, u32& st) {
    if (n >= 4) {
      int ks = 4 * (n - 4) + hw + crot; if (ks >= 43) ks -= 43;
      rw = mkrs(w2, F * S * 2); vo = (lane >> 3) * 4096 + cswz + stile * 128; so = ks * 131072; st = 32768;
    } else {
      rw = mkrs(wo, D * D * 2); vo = (lane >> 3) * 1024 + cswz + dtile * 128; so = ((4 * n + hw + orot) & 15) * 32768; st = 8192;
    }
  };
  auto c_issue = [&](int n) {
    rsrc_t rw; u32 vo, so, st;
    c_src(n, rw, vo, so, st);
    const u32 dst = ((6 + n) % 9) * 16384 + hw * 4096;
#pragma unroll
    for (int m = 0; m < 4; ++m) { if (n < NTW2) glds<true>(rw, vo, so + m * st, dst + m * 1024); else glds<false>(rw, vo, so + m * st, dst + m * 1024); }
  };
  const u32 aFULL = L_FULL + (lane & 3) * 4, aPROG = L_PROG + (lane & 3) * 4;
  int c_first = 0;
  u32 fullv = 0;
  auto pub = [&](u32 v) { if (v > fullv) { fullv = v; cw_st(L_FULL + hw * 4, v); } };

  if (roleA) {
    const int row0 = a_rb * 128, arot = r + 2 * (r >> 3);
    if (!isG) {
      const rsrc_t rw = mkrs(wqkv, D * 3 * D * 2);
      const u32 xvo = (row0 + 32 * hw + (lane >> 3)) * 1024, xsw = lane >> 4;
      const u32 wvo = (16 * hw + (lane >> 3)) * 3072 + cswz + a_grp * 128;
      const u32 xv0 = xvo + (((lane & 7) ^ xsw) << 4), xv1 = xvo + (((lane & 7) ^ (xsw + 4)) << 4);
      u32 dst = 0, cc = arot & 7;
      auto a_issue = [&]() {
#pragma unroll
        for (int i = 0; i < 4; ++i) glds<false>(rx, (i & 1) ? xv1 : xv0, i * 8192 + cc * 128, dst + hw * 4096 + i * 1024);
#pragma unroll
        for (int i = 0; i < 6; ++i) glds<false>(rw, wvo, (64 * cc + 8 * (i & 1)) * 3072 + (i >> 1) * 1024, dst + 16384 + (i >> 1) * 8192 + hw * 2048 + (i & 1) * 1024);
        dst = dst == 81920 ? 0 : dst + 40960; cc = (cc + 1) & 7;
      };
      a_issue();
      code_touch();
      if (EARLY) BARRIER();
      ST(13);
      VMCNT(1);
      BARRIER();
      a_issue(); a_issue();
#pragma unroll 1
      for (int c = 1; c < 6; ++c) { VMCNT(10); BARRIER(); a_issue(); }
      VMCNT(10);
      BARRIER();
      if (w == 4) {
        flag_wait<1>(ws, WS_FU, xc, ID_HALL, 0, tag);
        const u32x4 stin = ld128c(rws, WS_STAT + row0 * 8 + lane * 16, 0);
        *(LAS u32x2*)(L_AST + lane * 8) = u32x2{stin.x, stin.z};
        *(LAS u32x2*)(L_AST + 512 + lane * 8) = u32x2{stin.y, stin.w};
      }
      VMCNT(0);
      BARRIER();
      if (PTOUCH) {
        const rsrc_t rw2 = mkrs(w2, F * S * 2);
#pragma unroll
        for (int q = 1; q < 2; ++q) {
          u32 vo;
          if (!MAPC) { const int ln = (r * 3 + hw) * 64 + lane; vo = hw < 3 ? (ln >> 2) * 4096 + (xc + 4 * q) * 512 + (ln & 3) * 128 : ~0u; }
          else {
            const int pc = 6 + hw, ln = pc < 6 ? (r * 6 + pc) * 64 + lane : (((xc & 1) * 32 + r) * 3 + pc - 6) * 64 + lane;
            vo = pc < 9 ? (ln >> 3) * 4096 + ((xc >> 1) + (pc < 6 ? 0 : 2)) * 1024 + (ln & 7) * 128 : ~0u;
          }
          asm volatile("s_mov_b32 m0, %2\n\ts_nop 0\n\tbuffer_load_dword %0, %1, 0 offen lds" ::"v"(vo), "s"(rw2), "s"(L_DUMMY) : "memory");
        }
      }
      ST(2);
      BARRIER();
      c_issue(4); c_issue(5); c_issue(0); c_issue(1); c_issue(2); c_issue(3);
      c_first = 6;
      ST(3);
      if (OWAIT) lds_wait<true>(aPROG, 64); else wait_word(L_CNT + 4, 4);
    } else {
      const int rb2 = w & 1, hp = w >> 1;
      u32x4 wA0, wA1, sel0, selA, selB;
      const u32x4 z4 = {0, 0, 0, 0};
      {
        const u16* p = wphi + (a_grp * 4 + 2 * hp + blk) * 256 + 4 * hh * 16 + l15;
        const u32x4 wq = {(u32)p[0] | ((u32)p[16] << 16), (u32)p[32] | ((u32)p[48] << 16), (u32)p[128] | ((u32)p[144] << 16), (u32)p[160] | ((u32)p[176] << 16)};
        wA0 = blk ? z4 : wq; wA1 = blk ? wq : z4;
        const int dlt = l15 - 4 * hh;
        const u32x4 sel = {dlt == 0 ? 0x3f80u : dlt == 1 ? 0x3f800000u : 0u, dlt == 2 ? 0x3f80u : dlt == 3 ? 0x3f800000u : 0u,
                           dlt == 8 ? 0x3f80u : dlt == 9 ? 0x3f800000u : 0u, dlt == 10 ? 0x3f80u : dlt == 11 ? 0x3f800000u : 0u};
        sel0 = blk ? z4 : sel;
        selA = blk == rb2 ? sel : z4; selB = blk == rb2 ? z4 : sel;
        PIN(wA0); PIN(wA1); PIN(sel0); PIN(selA); PIN(selB);
      }
      f32x16 aq[2] = {zero16, zero16}, ak[2] = {zero16, zero16}, av[2] = {zero16, zero16};
      f32x4 cs3[3] = {{0, 0, 0, 0}, {0, 0, 0, 0}, {0, 0, 0, 0}};
      const u32 one2 = 0x3f803f80u, csel1 = l15 == blk ? one2 : 0u;
      const u32x4 csel = {csel1, csel1, csel1, csel1};
      const u32 xb0 = (64 * rb2 + l31) * 128 + ((hh ^ ((l31 >> 1) & 7)) << 4);
      const u32 wb0 = 16384 + (8 * hh + (l15 >> 2)) * 128 + ((hp ^ (l15 >> 3)) << 6) + blk * 32 + (l15 & 3) * 8;
      u32x4 xa[2][2] = {{z4, z4}, {z4, z4}}, wa[2][3] = {{z4, z4, z4}, {z4, z4, z4}};
      u32 xb, wb, slot = 0;
      auto a_step = [&](int kc, bool rd, int kr) {
#pragma unroll
        for (int q = 0; q < 6; ++q) {
          const int sec = q >> 1, a = q & 1;
          if (rd) {
            if (q < 2) xa[kr & 1][q] = ldsr128((xb ^ (kr * 32)) + q * 4096);
            if (q >= 2 && q < 5) { const u32 ad = wb + (q - 2) * 8192 + kr * 2048; wa[kr & 1][q - 2] = cat(trd(ad), trd(ad + 512)); }
          }
          SB();
          if (sec == 0) { aq[a] = mfma(wa[kc & 1][0], xa[kc & 1][a], aq[a]); PIN(aq[a]); }
          if (sec == 1) { ak[a] = mfma(wa[kc & 1][1], xa[kc & 1][a], ak[a]); PIN(ak[a]); }
          if (sec == 2) { av[a] = mfma(xa[kc & 1][a], wa[kc & 1][2], av[a]); PIN(av[a]); }
          SB();
        }
        if (rb2 == (kc & 1)) {
#pragma unroll
          for (int q = 0; q < 3; ++q) { cs3[q] = mfma16(csel, wa[kc & 1][q], cs3[q]); PIN(cs3[q]); }
        }
      };
      if (EARLY) BARRIER();
      ST(13);
#pragma unroll 1
      for (int c = 0; c < 8; ++c) {
        BARRIER();
        ST2R(16 + c);
        xb = opv(slot + xb0); wb = opv(slot + wb0);
        slot = slot == 81920 ? 0 : slot + 40960;
        a_step(3, true, 0); a_step(0, true, 1); a_step(1, true, 2); a_step(2, true, 3);
      }
      a_step(3, false, 0);
      if (lane < 16) {
        const u32 ca = L_ACS + hp * 384 + lane * 4;
#pragma unroll
        for (int q = 0; q < 3; ++q) { lds_addf(ca + q * 128, cs3[q][0]); lds_addf(ca + q * 128 + 64, cs3[q][1]); }
      }
      const u32 stb = opv(L_AST + (64 * rb2 + 4 * hh) * 4), stl = opv(L_AST + (64 * rb2 + l31) * 4);
      float sm[2], sr[2];
#pragma unroll
      for (int a = 0; a < 2; ++a) { sm[a] = *(LAS float*)(stl + a * 128); sr[a] = *(LAS float*)(stl + 512 + a * 128) * 1.4426950408889634f; }
      ST(2);
      BARRIER();
      auto feat = [&](const f32x16& acc, float m, const f32x4 (&c4)[4]) {
        u32 t[8];
        sfor<0, 8>([&](auto ic) { constexpr int k = decltype(ic)::v;
          t[k] = pk2v(pfma(sp2(-m), f32x2{c4[k >> 1][2 * (k & 1)], c4[k >> 1][2 * (k & 1) + 1]}, g2<k>(acc))); });
        return mfma(wA1, u32x4{t[4], t[5], t[6], t[7]}, mfma(wA0, u32x4{t[0], t[1], t[2], t[3]}, zero16));
      };
      const u32 csb = opv(L_ACS + hp * 384 + hh * 16);
      const u32 rec = (a_grp * 2 + hp) * 2 + rb2;
      {
        f32x4 ck[4];
#pragma unroll
        for (int i = 0; i < 4; ++i) ck[i] = ldsf4(csb + 128 + 32 * i);
        const float cv = *(LAS float*)(L_ACS + hp * 384 + 256 + l31 * 4);
        f32x16 pk[2];
#pragma unroll
        for (int a = 0; a < 2; ++a) pk[a] = feat(ak[a], sm[a], ck);
        f32x16 kv[2] = {zero16, zero16};
#pragma unroll
        for (int a = 0; a < 2; ++a) {
          f32x4 m4[4], r4[4];
#pragma unroll
          for (int i = 0; i < 4; ++i) { m4[i] = ldsf4(stb + a * 128 + 32 * i); r4[i] = ldsf4(stb + a * 128 + 512 + 32 * i); }
          u32x4 pos[2], neg[2];
          hedgehog(pk[a], sr[a], pos, neg);
          f32x16 t[2];
#pragma unroll
          for (int e = 0; e < 2; ++e) t[e] = mfma(neg[e], selB, mfma(pos[e], selA, zero16));
          u32 vb[8];
          sfor<0, 8>([&](auto ic) { constexpr int k = decltype(ic)::v;
            vb[k] = pk2v(pfma(f32x2{m4[k >> 1][2 * (k & 1)], m4[k >> 1][2 * (k & 1) + 1]}, sp2(-cv), g2<k>(av[a])) * f32x2{r4[k >> 1][2 * (k & 1)], r4[k >> 1][2 * (k & 1) + 1]}); });
#pragma unroll
          for (int e = 0; e < 2; ++e) {
            const bool own = blk == e;
            const u32x4 v0 = {own ? vb[0] : one2, own ? vb[1] : one2, own ? vb[2] : one2, own ? vb[3] : one2};
            const u32x4 v1 = {own ? vb[4] : one2, own ? vb[5] : one2, own ? vb[6] : one2, own ? vb[7] : one2};
            kv[e] = mfma(pk8<8>(t[e]), v1, mfma(pk8<0>(t[e]), v0, kv[e]));
          }
        }
        const u32 kab = L_AKV + w * 4352;
#pragma unroll
        for (int e = 0; e < 2; ++e) {
          if (blk == e || l15 == 0) {
            const u32 ka = opv(kab + (blk == e ? lane : 64 + e * 2 + hh) * 16);
            ldsw128(ka, get4<0>(kv[e])); ldsw128(ka + 1088, get4<4>(kv[e])); ldsw128(ka + 2176, get4<8>(kv[e])); ldsw128(ka + 3264, get4<12>(kv[e]));
          }
        }
        cw_st(L_SYN + w * 4, 1);
        const u32 zo = 1024 + ((l31 >> 2) & 1) * 1088 + (l31 >> 3) * 16 + (l31 & 3) * 4, kp = L_AKV + (w ^ 1) * 4352 + 2176;
        const f32x4 o0 = ldsf4(kab + lane * 16), o1 = ldsf4(kab + 1088 + lane * 16);
        const float oz = *(LAS float*)(kab + zo);
        ST(3);
        lds_wait<false>(L_SYN + (w ^ 1) * 4, 1);
        const f32x4 s0 = o0 + ldsf4(kp + lane * 16), s1 = o1 + ldsf4(kp + 1088 + lane * 16);
        const float zz = oz + *(LAS float*)(kp + zo);
        st128c(rws, WS_KV + (a_rb * 32 + rec) * 1024 + lane * 16, u32x4{pk2(s0[0], s0[1]), pk2(s0[2], s0[3]), pk2(s1[0], s1[1]), pk2(s1[2], s1[3])});
        if (lane < 32) __builtin_amdgcn_raw_buffer_store_b32(__float_as_uint(zz), rws, WS_Z + (a_rb * 32 + rec) * 128 + lane * 4, 0, 16);
      }
      u32x4 qp[2][2], qn[2][2];
      {
        f32x4 cq[4];
#pragma unroll
        for (int i = 0; i < 4; ++i) cq[i] = ldsf4(csb + 32 * i);
        const f32x16 p0 = feat(aq[0], sm[0], cq), p1 = feat(aq[1], sm[1], cq);
        publish(ws, WS_FKV, a_rb * 8 + a_grp, tag, L_CNT, 4, lane);
        ST(4);
        hedgehog(p0, sr[0], qp[0], qn[0]);
        hedgehog(p1, sr[1], qp[1], qn[1]);
#pragma unroll
        for (int a = 0; a < 2; ++a) { PIN(qp[a][0]); PIN(qp[a][1]); PIN(qn[a][0]); PIN(qn[a][1]); }
      }
      ST(5);
      if (w == 0) { flag_wait<1>(ws, WS_FKV, xc, l15 * 8 + a_grp, 0, tag); cw_st(L_LND, 1); }
      else lds_wait<false>(L_LND, 1);
      ST(6);
      u32x4 kmine;
      {
        u32x4 kvl[16]; u32 zl[16];
#pragma unroll
        for (int q = 0; q < 16; ++q) {
          kvl[q] = ld128c(rws, WS_KV + lane * 16, (q * 32 + rec) * 1024);
          zl[q] = __builtin_amdgcn_raw_buffer_load_b32(rws, WS_Z + l31 * 4, (q * 32 + rec) * 128, 16);
        }
        SB();
        f32x16 tot = zero16;
        float zz = 0.f;
#pragma unroll
        for (int q = 0; q < 16; ++q) { tot = mfma(sel0, kvl[q], tot); zz += __uint_as_float(zl[q]); }
        kmine = pk8<0>(tot);
        ldsw128(L_AT + w * 1024 + lane * 16, kmine);
        if (lane < 32) *(LAS u16*)(L_AZT + w * 64 + lane * 2) = (u16)pk2(zz, zz);
      }
      cw_st(L_SYN + w * 4, 2);
      lds_wait<false>(L_SYN + (w ^ 1) * 4, 2);
      ST(14);
      {
        const u32x4 k0 = ldsr128(L_AT + 2 * hp * 1024 + lane * 16), k1 = ldsr128(L_AT + (2 * hp + 1) * 1024 + lane * 16);
        const u32x4 y0 = ldsr128(L_AZT + 2 * hp * 64 + ((1 - blk) * 2 + hh) * 16), y1 = ldsr128(L_AZT + (2 * hp + 1) * 64 + ((1 - blk) * 2 + hh) * 16);
#pragma unroll
        for (int e = 0; e < 2; ++e) {
          const bool own = blk == e;
          const u32x4 a0 = own ? k0 : y0, a1 = own ? k1 : y1;
#pragma unroll
          for (int a = 0; a < 2; ++a) {
            const f32x16 nd = mfma(a1, qn[a][e], mfma(a0, qp[a][e], zero16));
            const float rd = __builtin_amdgcn_rcpf(nd[8 - 8 * e] + 1e-6f);
            const int o = 8 * e;
            st128c(rws, WS_O + ((a_grp * 4 + 2 * hp + e) * 64 + a_rb * 4 + 2 * rb2 + a) * 1024 + lane * 16,
                   u32x4{pk2(nd[o] * rd, nd[o + 1] * rd), pk2(nd[o + 2] * rd, nd[o + 3] * rd), pk2(nd[o + 4] * rd, nd[o + 5] * rd), pk2(nd[o + 6] * rd, nd[o + 7] * rd)});
          }
        }
        cw_st(L_PROG + w * 4, 64);
        publish(ws, WS_FO, a_rb * 8 + a_grp, tag, L_CNT + 4, 4, lane);
      }
      ST(7);
    }
  } else {
    const int rot = MAPB ? 8 * (b_dt + (r >> 3)) : 8 * b_dt + 4 * (r >> 2);
    if (isG) {
      const int li = (xc - 4) * 32 + r;
      const int rl = 4 * w + (lane >> 4), row = li * 16 + rl;
      u32x4 xr[4];
#pragma unroll
      for (int n = 0; n < 4; ++n) xr[n] = __builtin_amdgcn_raw_buffer_load_b128(rx, row * 1024 + l15 * 16 + n * 256, 0, 0);
      SB();
      if (EARLY) BARRIER();
      ST(13);
      float s1[2] = {0.f, 0.f}, s2[2] = {0.f, 0.f};
#pragma unroll
      for (int i = 0; i < 16; ++i) { const u32 v = xr[i >> 2][i & 3]; s1[i & 1] = dot2(v, 0x3f803f80u, s1[i & 1]); s2[i & 1] = dot2(v, v, s2[i & 1]); }
      ST(1);
      const float m = sum16(s1[0] + s1[1]) * (1.f / D);
      const float var = fmaxf(sum16(s2[0] + s2[1]) * (1.f / D) - m * m, 0.f);
      const float mb = bfr(m);
      const float ri = __builtin_amdgcn_rcpf(bfr(__builtin_amdgcn_sqrtf(bfr(bfr(var) + 1e-5f))));
      const float m2 = bfr((m - mb) * ri);
      const float r2 = __builtin_amdgcn_rcpf(bfr(__builtin_amdgcn_sqrtf(bfr(bfr(var * ri * ri) + 1e-5f))));
      const float ha = ri * r2, hb = -(mb * ri + m2) * r2;
      if (l15 == 0) *(LAS u32x2*)(L_LNS + rl * 8) = u32x2{__float_as_uint(mb), __float_as_uint(ri)};
      const u32 wa = opv(L_LN + rl * LN_PITCH + l15 * 16);
#pragma unroll
      for (int n = 0; n < 4; ++n) {
        u32x4 hv;
#pragma unroll
        for (int i = 0; i < 4; ++i) hv[i] = pk2v(pfma(unpk(xr[n][i]), sp2(ha), sp2(hb)));
        ldsw128(wa + n * 256, hv);
      }
      cw_st(L_SYN + w * 4, 1);
      ST(14);
      const u32 ad = opv(L_LN + (8 * hh + (l15 >> 2)) * LN_PITCH + w * 256 + blk * 32 + (l15 & 3) * 8);
      const u32 ho = WS_H + (li * 16 + 4 * w) * 1024 + lane * 16;
      lds_wait<false>(L_SYN + (lane & 3) * 4, 1);
#pragma unroll
      for (int rr = 0; rr < 4; ++rr) st128c(rws, ho + rr * 1024, cat(trd(ad + rr * 64), trd(ad + rr * 64 + 4 * LN_PITCH)));
      if (w == 0 && lane < 8) st128c(rws, WS_STAT + li * 128 + lane * 16, ldsr128(L_LNS + lane * 16));
      cw_st(L_SYN + w * 4, 2);
      ST(2);
      publish<1>(ws, WS_FH, li, tag, L_CNT + 12, 4, lane);
      ST(3);
    }
    if (isG) {
      if (roleB) {
        u32x2 bias[6];
#pragma unroll
        for (int t = 0; t < 6; ++t) { int f = b_ft * 96 + (t >> 1) * 32 + 16 * b_kz + 8 * (t & 1) + 4 * hh; if (f > F - 4) f = F - 4; bias[t] = *(const GAS u32x2*)(b1 + f); }
        f32x16 acc[3];
#pragma unroll
        for (int t = 0; t < 3; ++t) {
          acc[t] = zero16;
#pragma unroll
          for (int i = 0; i < 2; ++i) { acc[t][4 * i] = lo16(bias[2 * t + i].x); acc[t][4 * i + 1] = hi16(bias[2 * t + i].x); acc[t][4 * i + 2] = lo16(bias[2 * t + i].y); acc[t][4 * i + 3] = hi16(bias[2 * t + i].y); }
          PIN(acc[t]);
        }
        cw_st(L_PROG + w * 4, 32);
        if (w == 0) { flag_wait<1>(ws, WS_FU, xc, ID_HALL, 0, tag); cw_st(L_GO, 1); }
        else wait_word(L_GO, 1);
        ST(4);
        const u32 hv0 = WS_H + b_kz * (32 * 32768) + (b_dt * 4 + w) * 1024 + lane * 16, hv1 = hv0 + 16384;
        u32x4 hr[16];
        u32 hso = (rot & 31) * 32768;
        auto h_next = [&]() { hso = (hso + 32768) & (32 * 32768 - 1); };
#pragma unroll
        for (int i = 0; i < 8; ++i) { hr[2 * i] = ld128c(rws, hv0, hso); hr[2 * i + 1] = ld128c(rws, hv1, hso); h_next(); }
        SB();
        const u32 b0 = (8 * hh + (l15 >> 2)) * 192 + (blk ^ b_kz) * 32 + (l15 & 3) * 8;
        u32x4 wfr[2][6];
        u32 pos = 2, gb = opv(b0 + 2 * 24576);
        bool ready = true;
        auto b_rd = [&](int buf, int k, int t) {
          const u32 ad = gb + k * 6144 + (t / 3) * 3072 + (t % 3) * 64;
          wfr[buf][t] = cat(trd(ad), trd(ad + 768));
        };
        lds_wait<false>(aFULL, 9);
#pragma unroll
        for (int t = 0; t < 6; ++t) b_rd(0, 0, t);
        u32 progv = 32, need = 9;
        const u32 proga = L_PROG + w * 4;
        auto b_pair = [&](bool more) {
#pragma unroll
          for (int mi = 0; mi < 2; ++mi) {
            ST2R(7 + need);
            u32 fv, gbn;
#pragma unroll
            for (int k = 0; k < 4; ++k) {
              if (k == 3) {
                gb = gbn;
                if ((more || mi < 1) && __builtin_expect(!ready, 0)) lds_wait<false>(aFULL, need);
              }
#pragma unroll
              for (int t = 0; t < 6; ++t) {
                if (more || mi < 1 || k < 3) b_rd((k + 1) & 1, (k + 1) & 3, t);
                if (k == 2 && t == 1) fv = *(volatile LAS u32*)aFULL;
                if (k == 2 && t == 5) cw_st(proga, progv);
                SB();
                acc[t % 3] = mfma(wfr[k & 1][t], hr[(mi * 4 + k) * 2 + t / 3], acc[t % 3]);
                PIN(acc[t % 3]);
                SB();
                if (t == 2 && more) hr[(mi * 4 + k) * 2] = ld128c(rws, hv0, hso);
                if (t == 5 && more) { hr[(mi * 4 + k) * 2 + 1] = ld128c(rws, hv1, hso); h_next(); }
                if (k == 1 && t == 1) { progv += 4; PIN(progv); pos = pos == 5 ? 0 : pos + 1; ++need; }
                if (k == 1 && t == 3) gbn = opv(b0 + pos * 24576);
                if (k == 2 && t == 5) ready = __ballot(fv < need) == 0;
                SB();
              }
            }
          }
        };
#pragma unroll 1
        for (int mo = 0; mo < 3; ++mo) b_pair(true);
        b_pair(false);
        ST(5);
        const u32 pa = WS_P + w * 6144 + lane * 16;
        u32x4 oth[6];
        if (PTAG) {
          const u32 t32 = (u32)tag;
          const u32x4 tg4 = {t32 & 255u, (t32 >> 8) & 255u, (t32 >> 16) & 255u, t32 >> 24};
#pragma unroll
          for (int t = 0; t < 6; ++t) st128c(rws, pa + b_id * 24576 + t * 1024, (((t & 1) ? get4<12>(acc[t >> 1]) : get4<8>(acc[t >> 1])) & ~255u) | tg4);
          cw_add(L_CNT + 4, lane);
          ST(30);
          __builtin_amdgcn_s_sleep(PDELAY);
#pragma unroll 1
          for (u32 sp = 0; sp < SPIN_G; ++sp) {
#pragma unroll
            for (int t = 0; t < 6; ++t) oth[t] = ld128c(rws, pa + (b_id ^ 1) * 24576 + t * 1024, 0);
            const u32x4 d = ((oth[0] ^ tg4) | (oth[1] ^ tg4) | (oth[2] ^ tg4)) | ((oth[3] ^ tg4) | (oth[4] ^ tg4) | (oth[5] ^ tg4));
            if (__all((((d.x | d.y) | (d.z | d.w)) & 255u) == 0)) break;
            asm volatile("" ::: "memory");
          }
#pragma unroll
          for (int t = 0; t < 6; ++t) oth[t] &= ~255u;
          ST(31);
        } else {
#pragma unroll
          for (int t = 0; t < 3; ++t) {
            st128c(rws, pa + b_id * 24576 + 2 * t * 1024, get4<8>(acc[t]));
            st128c(rws, pa + b_id * 24576 + (2 * t + 1) * 1024, get4<12>(acc[t]));
          }
          publish<1>(ws, WS_FP, b_id, tag, L_CNT + 4, 4, lane);
          ST(30);
          if (w == 0) { flag_wait<1>(ws, WS_FP, 0, b_id ^ 1, 0, tag); cw_st(L_LND, 1); }
          else lds_wait<false>(L_LND, 1);
          ST(31);
#pragma unroll
          for (int t = 0; t < 6; ++t) oth[t] = ld128c(rws, pa + (b_id ^ 1) * 24576 + t * 1024, 0);
        }
        SB();
#pragma unroll
        for (int t = 0; t < 3; ++t) {
          u32x4 uv;
#pragma unroll
          for (int i = 0; i < 4; ++i) {
            const f32x2 v = f32x2{acc[t][2 * i], acc[t][2 * i + 1]} + f32x2{__uint_as_float(oth[2 * t + (i >> 1)][2 * (i & 1)]), __uint_as_float(oth[2 * t + (i >> 1)][2 * (i & 1) + 1])};
            const float c0 = -2.f * 0.7978845608028654f * 1.4426950408889634f;
            const f32x2 zz = v * pfma(v * v, sp2(0.044715f * c0), sp2(c0));
            uv[i] = pk2v(v * rc2(ex2(zz) + sp2(1.f)));
          }
          if (b_ft * 6 + 2 * t < 86) st128c(rws, WS_U + ((b_ft * 6 + 2 * t + b_kz) * 16 + b_dt * 4 + w) * 1024 + lane * 16, uv);
        }
        ST(6);
        publish(ws, WS_FU, b_id, tag, L_CNT, 4, lane);
        ST(7);
      } else {
        if (bid == 8 * 31 + 7 && w == 0) {
          flag_wait<2>(ws, WS_FH, 0, lane, lane + 64, tag);
          flag_set(ws, WS_FU, ID_HALL, tag, lane);
        }
        cw_st(L_PROG + w * 4, 64);
      }
    } else if (roleB) {
      const rsrc_t rw1 = mkrs(w1, S * F * 2);
      u32 vo[6];
      {
        u32 rr = (lane * 43) >> 9, ch = lane - rr * 12;
        const u32 cm = b_ft == 14 ? 3 : 15;
#pragma unroll
        for (int n = 0; n < 6; ++n) {
          vo[n] = rr * (F * 2) + (ch & cm) * 16 + b_ft * 192;
          ch += 4; rr += 5;
          if (ch >= 12) { ch -= 12; ++rr; }
        }
      }
      u32 pos = 2;
      auto b_issue = [&](int m) {
        const u32 so = (b_kz * 32 + ((4 * m + hw + rot) & 31)) * (32 * F * 2);
#pragma unroll
        for (int n = 0; n < 6; ++n) glds<false>(rw1, vo[n], so, pos * 24576 + hw * 6144 + n * 1024);
      };
      if (BPRE) b_issue(8);
      if (BPRE > 1) { pos = 3; b_issue(9); }
      if (EARLY) BARRIER();
      ST(13);
      if (HDRAIN) wait_word(L_CNT + 12, 4); else lds_wait<true>(L_SYN + (lane & 3) * 4, 2);
      if (HOLDGO == 8) wait_word(L_GO, 1);
      if (!BPRE) b_issue(8);
      code_touch();
      ST(16);
#pragma unroll 1
      for (int m = BPRE > 1 ? 10 : 9; m < 16; ++m) {
        pos = pos == 5 ? 0 : pos + 1;
        if (m == HOLDGO) { VMCNT(0); pub(m); wait_word(L_GO, 1); }
        if (m >= 14) { VMCNT(0); pub(m); lds_wait<true>(aPROG, 4 * (m - 5)); }
        b_issue(m);
        VMCNT(6 * BDEP);
        pub(m + 1 - BDEP);
        if (m == 13) ST(17);
      }
      ST(18);
      VMCNT(12); pub(14); VMCNT(6); pub(15); VMCNT(0); pub(16);
      ST(4);
    } else if (EARLY) {
      BARRIER();
    }
  }

  if (isG) {
    const int p = w & 1, kh = w >> 1;
    const int srow = stile * 64 + kh * 32 + l31;
    u32x2 xin[4];
#pragma unroll
    for (int t = 0; t < 4; ++t) xin[t] = *(const GAS u32x2*)(x + srow * D + dtile * 64 + p * 32 + 8 * t + 4 * hh);
    const u32 b2v = b2[srow];
    if (w == 0) {
      flag_wait<1>(ws, hh ? WS_FO : WS_FU, xc, hh ? (stile >> 1) * 8 + (lane & 7) : ((l31 < 30 ? l31 >> 1 : 0) * 4 + (dtile >> 1)) * 2 + (lane & 1), 0, tag);
      cw_st(L_GOO, 1);
    } else wait_word(L_GOO, 1);
    ST(8);
    const u32 uv0 = WS_U + (dtile * 2 + p) * 1024 + lane * 16, uv1 = uv0 + 16384, ov0 = WS_O + (stile * 2 + p) * 1024 + lane * 16, ov1 = ov0 + 65536;
    u32x4 ar[16], orr[16];
    u32 uso = (kh + crot) * 32768, oso = ((kh + orot) & 15) * 131072;
    auto a_load = [&](int i, int h) {
      if (i >= 8) {
        ar[(2 * i + h) & 15] = ld128c(rws, h ? uv1 : uv0, uso);
        if (h) { uso += 65536; if (uso >= 43 * 32768) uso -= 43 * 32768; }
      } else {
        orr[2 * i + h] = ld128c(rws, h ? ov1 : ov0, oso);
        if (h) oso = (oso + 262144) & (16 * 131072 - 1);
      }
    };
#pragma unroll
    for (int i = 0; i < 8; ++i) { a_load(i, 0); a_load(i, 1); }
    SB();
    f32x16 ac[2] = {zero16, zero16};
    u32 tb[2], tg[2];
#pragma unroll
    for (int t = 0; t < 2; ++t) tb[t] = (4 * hh + (l15 >> 2)) * 128 + ((t ^ kh ^ (l15 >> 3)) << 6) + blk * 32 + (l15 & 3) * 8 + kh * 4096;
    u32x4 wfr[2][4];
    auto c_rd = [&](auto ic, int t) {
      constexpr int i = decltype(ic)::v;
      if (i >= 30) return;
      const u32 ad = tg[t & 1] + (i & 1) * 8192 + (t >> 1) * 2048;
      wfr[i & 1][t] = cat(trd(ad), trd(ad + 1024));
    };
    lds_wait<false>(aFULL, 17);
    tg[0] = opv(tb[0] + 6 * 16384); tg[1] = opv(tb[1] + 6 * 16384);
#pragma unroll
    for (int t = 0; t < 4; ++t) c_rd(IC<0>{}, t);
    u32 fv, tn[2], progv = 64;
    u32x4 skeep[2];
    bool ready = true;
    f32x2 sv[8];
    const u32 proga = L_PROG + w * 4;
    sfor<0, 30>([&](auto ic) {
      constexpr int i = decltype(ic)::v;
      constexpr bool ld = i + 8 < 30;
      if ((i & 1) == 0) ST2(32 + i / 2);
      if (i == 8) {
        ST(9);
        const u32x4 a0 = pk8<0>(ac[0]), a1 = pk8<8>(ac[0]), b0 = pk8<0>(ac[1]), b1 = pk8<8>(ac[1]);
        const u32 sa = opv(L_CS + w * 2048 + lane * 16), sb = opv(L_CS + (4 + p) * 2048 + lane * 16);
        ldsw128(sa, b0); ldsw128(sa + 1024, b1);
        if (kh != p) { ldsw128(sb, a0); ldsw128(sb + 1024, a1); }
        skeep[0] = a0; skeep[1] = a1;
        cw_st(L_SYN + w * 4, 6);
        ac[1] = zero16;
#pragma unroll
        for (int q = 0; q < 16; ++q) ac[0][q] = lo16(b2v);
        ST(15);
      }
      if (i == 14) {
        lds_wait<false>(L_SYN + (lane & 3) * 4, 6);
        const u32 s1 = L_CS + (kh + 2 * (1 - p)) * 2048 + lane * 16, s2 = L_CS + (4 + kh) * 2048 + lane * 16;
        const u32x4 c0 = ldsr128(s1), c1 = ldsr128(s1 + 1024);
        u32x4 d0 = skeep[0], d1 = skeep[1];
        if (kh != p) { d0 = ldsr128(s2); d1 = ldsr128(s2 + 1024); }
#pragma unroll
        for (int q = 0; q < 4; ++q) { sv[q] = unpk(c0[q]) + unpk(d0[q]); sv[4 + q] = unpk(c1[q]) + unpk(d1[q]); PIN(sv[q]); PIN(sv[4 + q]); }
      }
      if (i == 29 && kh == 1) { ar[10] = u32x4{0, 0, 0, 0}; ar[11] = ar[10]; }
      if ((i & 1) && i < 29) {
        tg[0] = tn[0]; tg[1] = tn[1];
        if (__builtin_expect(!ready, 0)) lds_wait<false>(aFULL, 16 + i / 2 + 2);
      }
#pragma unroll
      for (int t = 0; t < 4; ++t) {
        c_rd(IC<i + 1>{}, t);
        if (!(i & 1) && t == 0) fv = *(volatile LAS u32*)(i < 28 ? aFULL : aPROG);
        if (!(i & 1) && t == 3) cw_st(proga, progv);
        SB();
        if (i >= 8) ac[t & 1] = mfma(ar[(2 * i + (t >> 1)) & 15], wfr[i & 1][t], ac[t & 1]);
        else ac[t & 1] = mfma(wfr[i & 1][t], orr[(2 * i + (t >> 1)) & 15], ac[t & 1]);
        PIN(ac[t & 1]);
        SB();
        if (ld && (t & 1)) a_load(i + 8, t >> 1);
        if (!(i & 1) && t == 0) { progv += 4; PIN(progv); tn[0] = opv(tb[0] + ((7 + i / 2) % 9) * 16384); tn[1] = opv(tb[1] + ((7 + i / 2) % 9) * 16384); }
        if (!(i & 1) && t == 3) ready = __ballot(fv < (i < 28 ? 16 + i / 2 + 2 : 64 + 36)) == 0;
        SB();
      }
    });
    ST(10);
    if (!ready) lds_wait<false>(aPROG, 64 + 36);
    const u32 ya = opv(L_CY + w * 4096 + lane * 16);
    ldsw128(ya, get4<0>(ac[1])); ldsw128(ya + 1024, get4<4>(ac[1])); ldsw128(ya + 2048, get4<8>(ac[1])); ldsw128(ya + 3072, get4<12>(ac[1]));
    cw_st(L_SYN + w * 4, 7);
    lds_wait<false>(L_SYN + (w ^ 2) * 4, 7);
    f32x4 yo[4];
#pragma unroll
    for (int t = 0; t < 4; ++t) yo[t] = ldsf4((ya ^ 8192) + t * 1024);
    SB();
#pragma unroll
    for (int t = 0; t < 4; ++t) {
      u32x2 ov;
#pragma unroll
      for (int i = 0; i < 2; ++i) {
        const f32x2 yy = f32x2{ac[0][4 * t + 2 * i], ac[0][4 * t + 2 * i + 1]} + f32x2{yo[t][2 * i], yo[t][2 * i + 1]};
        const f32x2 gg = yy * rc2(ex2(yy * sp2(-1.4426950408889634f)) + sp2(1.f));
        ov[i] = pk2v(pfma(sv[2 * t + i], gg, unpk(xin[t][i])));
      }
      *(GAS u32x2*)(out + srow * D + dtile * 64 + p * 32 + 8 * t + 4 * hh) = ov;
    }
  } else {
    u32x4 rc[6][4];
    if (!roleA) lds_wait<true>(aPROG, CHOLD);
#pragma unroll
    for (int n = 0; n < 9; ++n) {
      if (n >= c_first) {
        if (n > 2 && n < 8) lds_wait<true>(aPROG, c_need(n));
        if (n == HOLDN && roleB) wait_word(HOLDU ? L_CNT : L_CNT + 4, 4);
        c_issue(n);
      }
      ST2(32 + n);
    }
    VMCNT(12); pub(16 + 6);
#pragma unroll
    for (int q = 9; q < 15; ++q) {
      rsrc_t rw; u32 vo2, so2, st2;
      c_src(q, rw, vo2, so2, st2);
#pragma unroll
      for (int m = 0; m < 4; ++m) rc[q - 9][m] = __builtin_amdgcn_raw_buffer_load_b128(rw, vo2, so2 + m * st2, 0);
    }
    SB();
    VMCNT(24); pub(16 + 9);
    ST(8);
    ST(9);
    const u32 wa = hw * 4096 + lane * 16;
#pragma unroll
    for (int n = 9; n < 15; ++n) {
      lds_wait<true>(aPROG, c_need(n));
      if (CSTAGE) {
        const u32 wb = opv(wa + ((6 + n) % 9) * 16384);
#pragma unroll
        for (int m = 0; m < 4; ++m) ldsw128(wb + m * 1024, rc[n - 9][m]);
        pub(16 + n + 1);
      } else {
        c_issue(n);
        if (n >= 12) { VMCNT(8); pub(16 + n - 1); }
        if (n == 14) { VMCNT(4); pub(30); VMCNT(0); pub(31); }
      }
      ST2(32 + n);
    }
    if (w == 4 && bid == 0) {
      if (lane == 0) __hip_atomic_store((GAS u64*)(ws + WS_EPOCH), tag, __ATOMIC_RELAXED, __HIP_MEMORY_SCOPE_AGENT);
    }
  }
#endif
}
#undef tag
}

extern "C" void kernel_launch(void* const* d_in, const int* in_sizes, int n_in, void* d_out, int out_size, void* d_ws, size_t ws_size,
                              hipStream_t stream) {
  using namespace hk;
  (void)hipFuncSetAttribute((const void*)fused, hipFuncAttributeMaxDynamicSharedMemorySize, LDS_BYTES);
  fused<<<256, 512, LDS_BYTES, stream>>>((const u16*)d_in[0], (const u16*)d_in[1], (const u16*)d_in[2], (const u16*)d_in[3], (const u16*)d_in[5],
                                         (const u16*)d_in[6], (const u16*)d_in[7], (const u16*)d_in[8], (u16*)d_out, (char*)d_ws);
}
```

```cpp
#include <hip/hip_runtime.h>
#include <stdint.h>

namespace hk {
typedef unsigned short u16;
typedef unsigned u32;
typedef unsigned long long u64;
typedef u32 u32x4 __attribute__((ext_vector_type(4)));
typedef u32 u32x2 __attribute__((ext_vector_type(2)));
typedef float f32x4 __attribute__((ext_vector_type(4)));
typedef float f32x2 __attribute__((ext_vector_type(2)));
typedef float f32x16 __attribute__((ext_vector_type(16)));
typedef __bf16 bf16x8 __attribute__((ext_vector_type(8)));
typedef __bf16 bf16x2 __attribute__((ext_vector_type(2)));
typedef short s16x4 __attribute__((ext_vector_type(4)));
#define LAS __attribute__((address_space(3)))
#define GAS __attribute__((address_space(1)))
#define DI __device__ __forceinline__
typedef __amdgpu_buffer_rsrc_t rsrc_t;

#define BPRE 0
#define TOUCHN 1
#define RCUT 12
#define BCUT 9
#define BMODE 0
#define CHOLD 48
#define PSLEEP 0
constexpr int S = 2048, D = 512, F = 1376;
constexpr u32 WS_EPOCH = 0;
constexpr u32 FSTR = 128;
constexpr u32 FL_SZ = 8 * 128 * 128;
constexpr u32 WS_FH = 4096, WS_FKV = WS_FH + FL_SZ, WS_FO = WS_FKV + FL_SZ, WS_FU = WS_FO + FL_SZ, WS_FP = WS_FH + 128 * 128, WS_FC = WS_FP + 128 * 128;
constexpr int ID_HALL = 127;
constexpr u32 WS_STAT = WS_FU + FL_SZ;
constexpr u32 WS_CS = WS_STAT + S * 8;
constexpr u32 WS_Z = WS_CS + 128 * 768;
constexpr u32 WS_KV = WS_Z + 16 * 32 * 128;
constexpr u32 WS_H = WS_KV + 16 * 32 * 1024;
constexpr u32 WS_O = WS_H + S * D * 2;
constexpr u32 WS_U = WS_O + S * D * 2;
constexpr u32 WS_P = WS_U + D * F * 2;
constexpr u32 WS_END = WS_P + 120 * 24576;
constexpr u32 WS_STAMP = 64u << 20;
constexpr u32 RING = 147456, L_SCR = RING, L_CTL = 163840 - 256, LDS_BYTES = 163840;
constexpr u32 L_FULL = L_CTL, L_PROG = L_CTL + 16, L_GO = L_CTL + 32, L_GOO = L_CTL + 36, L_LND = L_CTL + 40, L_CNT = L_CTL + 48, L_SYN = L_CTL + 96;
constexpr u32 L_LN = 122880, LN_PITCH = 1088, L_LNS = L_LN + 16 * LN_PITCH, L_DUMMY = L_CTL - 256;
constexpr u32 L_ACP = L_CTL - 1024, L_ACS = L_SCR + 9216, L_AST = L_SCR + 8192;
constexpr u32 L_AKV = 49152, L_AT = L_AKV + 8 * 4352, L_AZT = L_AT + 4096;
constexpr u32 L_BX = 49152, L_CY = 65536, L_CS = 98304;
constexpr u32 SPIN_G = 1u << 15, SPIN_L = 1u << 20;

DI f32x16 mfma(u32x4 a, u32x4 b, f32x16 c) {
  return __builtin_amdgcn_mfma_f32_32x32x16_bf16(__builtin_bit_cast(bf16x8, a), __builtin_bit_cast(bf16x8, b), c, 0, 0, 0);
}
DI u32 pk2(float a, float b) { f32x2 v = {a, b}; return __builtin_bit_cast(u32, __builtin_convertvector(v, bf16x2)); }
DI float lo16(u32 p) { return __uint_as_float(p << 16); }
DI float hi16(u32 p) { return __uint_as_float(p & 0xffff0000u); }
DI float bfr(float a) { return lo16(pk2(a, a)); }
template <int O> DI u32x4 pk8(const f32x16& v) { return u32x4{pk2(v[O], v[O + 1]), pk2(v[O + 2], v[O + 3]), pk2(v[O + 4], v[O + 5]), pk2(v[O + 6], v[O + 7])}; }
template <int O> DI u32x4 get4(const f32x16& v) { return __builtin_bit_cast(u32x4, f32x4{v[O], v[O + 1], v[O + 2], v[O + 3]}); }
DI u32x2 trd(u32 addr) { return __builtin_bit_cast(u32x2, __builtin_amdgcn_ds_read_tr16_b64_v4i16((LAS s16x4*)addr)); }
DI u32x4 cat(u32x2 a, u32x2 b) { return u32x4{a.x, a.y, b.x, b.y}; }
DI u32x4 ldsr128(u32 addr) { return *(LAS u32x4*)addr; }
DI f32x4 ldsf4(u32 addr) { return *(LAS f32x4*)addr; }
DI void ldsw128(u32 addr, u32x4 v) { *(LAS u32x4*)addr = v; }
DI void lds_addf(u32 addr, float v) { (void)__hip_atomic_fetch_add((LAS float*)addr, v, __ATOMIC_RELAXED, __HIP_MEMORY_SCOPE_WORKGROUP); }
DI rsrc_t mkrs(const void* p, u32 bytes) { return __builtin_amdgcn_make_buffer_rsrc((void*)p, 0, bytes, 0x00020000); }
DI u32x4 ld128c(rsrc_t r, u32 vo, u32 so) { return __builtin_amdgcn_raw_buffer_load_b128(r, vo, so, 16); }
DI void st128c(rsrc_t r, u32 vo, u32x4 v) { __builtin_amdgcn_raw_buffer_store_b128(v, r, vo, 0, 16); }
DI void glds(rsrc_t r, u32 vo, u32 so, u32 lds) {
  asm volatile("s_mov_b32 m0, %3\n\ts_nop 0\n\tbuffer_load_dwordx4 %0, %1, %2 offen lds" ::"v"(vo), "s"(r), "s"(so), "s"(lds) : "memory");
}
#define VMCNT(n) asm volatile("s_waitcnt vmcnt(%0)" ::"i"(n) : "memory")
#define BARRIER() asm volatile("s_waitcnt lgkmcnt(0)\n\ts_barrier" ::: "memory")
#define SB() __builtin_amdgcn_sched_barrier(0)
#define PIN(x) asm volatile("" : "+v"(x))
DI u32 opv(u32 v) { PIN(v); return v; }
template <int CTRL> DI float dppf(float v) { return __uint_as_float(__builtin_amdgcn_update_dpp(0u, __float_as_uint(v), CTRL, 0xf, 0xf, true)); }
DI float fmax2(float a, float b) { return __builtin_elementwise_maximum(a, b); }
DI float fmin2(float a, float b) { return __builtin_elementwise_minimum(a, b); }
DI f32x4 mfma16(u32x4 a, u32x4 b, f32x4 c) {
  return __builtin_amdgcn_mfma_f32_16x16x32_bf16(__builtin_bit_cast(bf16x8, a), __builtin_bit_cast(bf16x8, b), c, 0, 0, 0);
}
DI f32x2 pfma(f32x2 a, f32x2 b, f32x2 c) { return __builtin_elementwise_fma(a, b, c); }
DI f32x2 sp2(float a) { return f32x2{a, a}; }
DI f32x2 ex2(f32x2 a) { return f32x2{__builtin_amdgcn_exp2f(a.x), __builtin_amdgcn_exp2f(a.y)}; }
DI f32x2 rc2(f32x2 a) { return f32x2{__builtin_amdgcn_rcpf(a.x), __builtin_amdgcn_rcpf(a.y)}; }
DI u32 pk2v(f32x2 v) { return __builtin_bit_cast(u32, __builtin_convertvector(v, bf16x2)); }
DI f32x2 unpk(u32 p) { return f32x2{lo16(p), hi16(p)}; }
template <int I> DI f32x2 g2(const f32x16& v) { return f32x2{v[2 * I], v[2 * I + 1]}; }
DI void swap16(float& a, float& b) { auto r = __builtin_amdgcn_permlane16_swap(__float_as_uint(a), __float_as_uint(b), false, false); a = __uint_as_float(r[0]); b = __uint_as_float(r[1]); }
DI void swap32(float& a, float& b) { auto r = __builtin_amdgcn_permlane32_swap(__float_as_uint(a), __float_as_uint(b), false, false); a = __uint_as_float(r[0]); b = __uint_as_float(r[1]); }
DI float sum16(float v) { v += dppf<0xB1>(v); v += dppf<0x4E>(v); v += dppf<0x141>(v); v += dppf<0x140>(v); return v; }
template <bool MAX> DI void xred2(float& a, float& b) {
  swap32(a, b);
  a = MAX ? fmax2(a, b) : a + b;
  b = a; swap32(a, b);
}
DI float dot2(u32 a, u32 b, float c) { return __builtin_amdgcn_fdot2_f32_bf16(__builtin_bit_cast(bf16x2, a), __builtin_bit_cast(bf16x2, b), c, false); }
template <int V> struct IC { static constexpr int v = V; };
template <int I, int N, class Fn> DI void sfor(Fn&& f) { if constexpr (I < N) { f(IC<I>{}); sfor<I + 1, N>(f); } }
DI void cw_st(u32 addr, u32 val) { asm volatile("ds_write_b32 %0, %1" ::"v"(addr), "v"(val) : "memory"); }
template <bool SLEEP> DI void lds_wait(u32 addr, u32 need) {
  u32 v, cnt = SPIN_L;
  if (SLEEP)
    asm volatile("s_branch 2f\n1:\n\ts_sleep 2\n2:\n\tds_read_b32 %0, %2\n\ts_waitcnt lgkmcnt(0)\n\tv_cmp_gt_u32 vcc, %3, %0\n\ts_sub_u32 %1, %1, 1\n\t"
                 "s_cselect_b64 vcc, 0, vcc\n\ts_cmp_lg_u64 vcc, 0\n\ts_cbranch_scc1 1b" : "=&v"(v), "+s"(cnt) : "v"(addr), "s"(need) : "vcc", "scc", "memory");
  else
    asm volatile("1:\n\tds_read_b32 %0, %2\n\ts_waitcnt lgkmcnt(0)\n\tv_cmp_gt_u32 vcc, %3, %0\n\ts_sub_u32 %1, %1, 1\n\t"
                 "s_cselect_b64 vcc, 0, vcc\n\ts_cmp_lg_u64 vcc, 0\n\ts_cbranch_scc1 1b" : "=&v"(v), "+s"(cnt) : "v"(addr), "s"(need) : "vcc", "scc", "memory");
}
DI bool lds_test(u32 addr, u32 need) {
  u32 v; asm volatile("ds_read_b32 %0, %1\n\ts_waitcnt lgkmcnt(0)" : "=v"(v) : "v"(addr) : "memory");
  return __ballot(v < need) == 0;
}
DI void wait_word(u32 addr, u32 need) { lds_wait<true>(addr, need); }
DI u32 cw_add(u32 addr, int lane) {
  u32 old = 0;
  if (lane == 0) asm volatile("ds_add_rtn_u32 %0, %1, %2\n\ts_waitcnt lgkmcnt(0)" : "=v"(old) : "v"(addr), "v"(1u) : "memory");
  return __builtin_amdgcn_readfirstlane(old);
}
template <int NREP = 8> DI void flag_set(char* ws, u32 base, int id, u64 tg, int lane) {
  if (lane < NREP) __hip_atomic_store((GAS u64*)(ws + base + (lane * 128 + id) * FSTR), tg, __ATOMIC_RELAXED, __HIP_MEMORY_SCOPE_AGENT);
}
template <int N> DI void flag_wait(char* ws, u32 base, int rep, int id0, int id1, u64 tg) {
  GAS u64* p0 = (GAS u64*)(ws + base + (rep * 128 + id0) * FSTR);
  GAS u64* p1 = (GAS u64*)(ws + base + (rep * 128 + id1) * FSTR);
#pragma unroll 1
  for (u32 sp = 0; sp < SPIN_G; ++sp) {
    bool ok = __hip_atomic_load(p0, __ATOMIC_RELAXED, __HIP_MEMORY_SCOPE_AGENT) == tg;
    if (N > 1) ok &= __hip_atomic_load(p1, __ATOMIC_RELAXED, __HIP_MEMORY_SCOPE_AGENT) == tg;
    if (__all(ok)) break;
    __builtin_amdgcn_s_sleep(PSLEEP);
  }
  __builtin_amdgcn_fence(__ATOMIC_ACQUIRE, "wavefront");
  asm volatile("" ::: "memory");
}
template <int NREP = 8> DI void publish(char* ws, u32 base, int id, u64 tg, u32 cnt, u32 nw, int lane) {
  VMCNT(0);
  if (cw_add(cnt, lane) == nw - 1) flag_set<NREP>(ws, base, id, tg, lane);
}

#define CT(i)
#define ST(i)
#define ST2(i)
#define ST2R(i)

DI void hedgehog(const f32x16& p, float sc, u32x4 (&pos)[2], u32x4 (&neg)[2]) {
  float mx[2], mn[2];
#pragma unroll
  for (int e = 0; e < 2; ++e) {
    const int o = 8 * e;
    mx[e] = fmax2(fmax2(fmax2(fmax2(p[o], p[o + 1]), p[o + 2]), fmax2(fmax2(p[o + 3], p[o + 4]), p[o + 5])), fmax2(p[o + 6], p[o + 7]));
    mn[e] = -fmin2(fmin2(fmin2(fmin2(p[o], p[o + 1]), p[o + 2]), fmin2(fmin2(p[o + 3], p[o + 4]), p[o + 5])), fmin2(p[o + 6], p[o + 7]));
  }
  xred2<true>(mx[0], mn[0]); xred2<true>(mx[1], mn[1]);
  f32x2 ep[8], en[8];
  sfor<0, 8>([&](auto ic) {
    constexpr int k = decltype(ic)::v;
    ep[k] = pfma(g2<k>(p), sp2(sc), sp2(-mx[k >> 2] * sc));
    en[k] = pfma(g2<k>(p), sp2(-sc), sp2(-mn[k >> 2] * sc));
  });
#pragma unroll
  for (int k = 0; k < 8; ++k) { ep[k] = ex2(ep[k]); en[k] = ex2(en[k]); }
#pragma unroll
  for (int e = 0; e < 2; ++e) {
    const int o = 4 * e;
    const f32x2 a = (ep[o] + ep[o + 1]) + (ep[o + 2] + ep[o + 3]), b = (en[o] + en[o + 1]) + (en[o + 2] + en[o + 3]);
    float sp = a.x + a.y, sn = b.x + b.y;
    xred2<false>(sp, sn);
    const f32x2 rs = sp2(__builtin_amdgcn_rcpf(sp)), rn = sp2(__builtin_amdgcn_rcpf(sn));
#pragma unroll
    for (int k = 0; k < 4; ++k) { pos[e][k] = pk2v(ep[o + k] * rs); neg[e][k] = pk2v(en[o + k] * rn); }
  }
}

constexpr int c_need(int n) { return n < 9 ? (n <= 2 ? 48 : n == 3 ? 52 : n <= 5 ? 56 : n == 6 ? 60 : 64) : 64 + 4 * (n - 8); }

__global__ __launch_bounds__(512, 2) void fused(const u16* __restrict__ x, const u16* __restrict__ wqkv, const u16* __restrict__ wphi,
                                                const u16* __restrict__ wo, const u16* __restrict__ w1, const u16* __restrict__ b1,
                                                const u16* __restrict__ w2, const u16* __restrict__ b2, u16* __restrict__ out, char* ws) {
#if __HIP_DEVICE_COMPILE__
  ST(0);
  const int tid = threadIdx.x, lane = tid & 63, w = __builtin_amdgcn_readfirstlane(tid >> 6);
  const int l15 = lane & 15, l31 = lane & 31, hh = lane >> 5, blk = (lane >> 4) & 1;
  const int bid = blockIdx.x, xc = bid & 7, r = bid >> 3;
  const bool isG = w < 4;
  const int hw = w & 3;
  const int stile = 8 * (xc >> 1) + (r & 7), dtile = 4 * (xc & 1) + (r >> 3);
  const bool roleA = r < 16;
  const int j = r & 15;
  const int b_dt = r & 3, b_kz = xc & 1, b_ft = 4 * (xc >> 1) + (j >> 2);
  const int b_id = (b_ft * 4 + b_dt) * 2 + b_kz;
  const bool roleB = !roleA && b_ft < 15;
  const int a_rb = 4 * (xc >> 1) + (j & 3), a_grp = 4 * (xc & 1) + (j >> 2);
  const rsrc_t rws = mkrs(ws, WS_END);
  const rsrc_t rx = mkrs(x, S * D * 2);
  const f32x16 zero16 = {0, 0, 0, 0, 0, 0, 0, 0, 0, 0, 0, 0, 0, 0, 0, 0};

  const u64 epoch = __hip_atomic_load((GAS u64*)(ws + WS_EPOCH), __ATOMIC_RELAXED, __HIP_MEMORY_SCOPE_AGENT);
#define tag (epoch + 1)
  if (tid < 320) *(LAS u32*)(L_CTL - 1024 + tid * 4) = 0;
  BARRIER();
  ST(12);
  const u32 cswz = ((lane & 7) ^ (blk << 2)) << 4;
  const int crot = ((((r >> 3) * 2 + (r & 7)) & 7) * 43) >> 3, orot = 2 * ((r + 2 * (r >> 3)) & 7);
  auto c_src = [&](int n, rsrc_t& rw, u32& vo, u32& so, u32& st) {
    if (n < 11) {
      int ks = 4 * n + hw + crot; if (ks >= 43) ks -= 43;
      rw = mkrs(w2, F * S * 2); vo = (lane >> 3) * 4096 + cswz + stile * 128; so = ks * 131072; st = 32768;
    } else {
      rw = mkrs(wo, D * D * 2); vo = (lane >> 3) * 1024 + cswz + dtile * 128; so = ((4 * (n - 11) + hw + orot) & 15) * 32768; st = 8192;
    }
  };
  auto c_issue = [&](int n) {
    rsrc_t rw; u32 vo, so, st;
    c_src(n, rw, vo, so, st);
    const u32 dst = ((6 + n) % 9) * 16384 + hw * 4096;
#pragma unroll
    for (int m = 0; m < 4; ++m) glds(rw, vo, so + m * st, dst + m * 1024);
  };
  const u32 aFULL = L_FULL + (lane & 3) * 4, aPROG = L_PROG + (lane & 3) * 4;
  u32 fullv = 0;
  auto pub = [&](u32 v) { if (v > fullv) { fullv = v; cw_st(L_FULL + hw * 4, v); } };

  if (roleA) {
    const int row0 = a_rb * 128, arot = 2 * (j + (j >> 2));
    if (!isG) {
      const rsrc_t rw = mkrs(wqkv, D * 3 * D * 2);
      const u32 xvo = (row0 + 32 * hw + (lane >> 3)) * 1024, xsw = lane >> 4;
      const u32 wvo = (16 * hw + (lane >> 3)) * 3072 + cswz + a_grp * 128;
      const u32 xv0 = xvo + (((lane & 7) ^ xsw) << 4), xv1 = xvo + (((lane & 7) ^ (xsw + 4)) << 4);
      u32 dst = 0, cc = arot & 7;
      auto a_issue = [&]() {
#pragma unroll
        for (int i = 0; i < 4; ++i) glds(rx, (i & 1) ? xv1 : xv0, i * 8192 + cc * 128, dst + hw * 4096 + i * 1024);
#pragma unroll
        for (int i = 0; i < 6; ++i) glds(rw, wvo, (64 * cc + 8 * (i & 1)) * 3072 + (i >> 1) * 1024, dst + 16384 + (i >> 1) * 8192 + hw * 2048 + (i & 1) * 1024);
        dst = dst == 81920 ? 0 : dst + 40960; cc = (cc + 1) & 7;
      };
      a_issue();
      ST(13);
      VMCNT(0);
      BARRIER();
      a_issue(); a_issue();
#pragma unroll 1
      for (int c = 1; c < 6; ++c) { VMCNT(10); BARRIER(); a_issue(); }
      VMCNT(10);
      BARRIER();
      VMCNT(0);
      BARRIER();
      if (w == 4) {
        flag_wait<1>(ws, WS_FU, xc, ID_HALL, 0, tag);
        const u32x4 stin = ld128c(rws, WS_STAT + row0 * 8 + lane * 16, 0);
        *(LAS u32x2*)(L_AST + lane * 8) = u32x2{stin.x, stin.z};
        *(LAS u32x2*)(L_AST + 512 + lane * 8) = u32x2{stin.y, stin.w};
      }
      if (w == 5) {
        flag_wait<1>(ws, WS_FC, 0, ((a_rb & ~3) + (lane & 3)) * 8 + a_grp, 0, tag);
        u32x4 gat[4];
#pragma unroll
        for (int q = 0; q < 4; ++q) gat[q] = ld128c(rws, WS_CS + (lane < 48 ? lane : 0) * 16, (((a_rb & ~3) + q) * 8 + a_grp) * 768);
        const f32x4 t = (__builtin_bit_cast(f32x4, gat[0]) + __builtin_bit_cast(f32x4, gat[1])) + (__builtin_bit_cast(f32x4, gat[2]) + __builtin_bit_cast(f32x4, gat[3]));
        if (lane < 48) ldsw128(L_ACS + lane * 16, __builtin_bit_cast(u32x4, t));
      }
      ST(2);
      BARRIER();
      const rsrc_t rw2 = mkrs(w2, F * S * 2);
#pragma unroll
      for (int q = 0; q < TOUCHN; ++q) {
        const int ln = ((j * 3 + q) * 4 + hw) * 64 + lane;
        asm volatile("s_mov_b32 m0, %2\n\ts_nop 0\n\tbuffer_load_dword %0, %1, 0 offen lds" ::"v"((ln >> 3) * 4096 + (xc >> 1) * 1024 + (ln & 7) * 128), "s"(rw2), "s"(L_DUMMY) : "memory");
      }
    } else {
      const int rb2 = w & 1, hp = w >> 1;
      u32x4 wA0, wA1, sel0, selA, selB;
      const u32x4 z4 = {0, 0, 0, 0};
      {
        const u16* p = wphi + (a_grp * 4 + 2 * hp + blk) * 256 + 4 * hh * 16 + l15;
        const u32x4 wq = {(u32)p[0] | ((u32)p[16] << 16), (u32)p[32] | ((u32)p[48] << 16), (u32)p[128] | ((u32)p[144] << 16), (u32)p[160] | ((u32)p[176] << 16)};
        wA0 = blk ? z4 : wq; wA1 = blk ? wq : z4;
        const int dlt = l15 - 4 * hh;
        const u32x4 sel = {dlt == 0 ? 0x3f80u : dlt == 1 ? 0x3f800000u : 0u, dlt == 2 ? 0x3f80u : dlt == 3 ? 0x3f800000u : 0u,
                           dlt == 8 ? 0x3f80u : dlt == 9 ? 0x3f800000u : 0u, dlt == 10 ? 0x3f80u : dlt == 11 ? 0x3f800000u : 0u};
        sel0 = blk ? z4 : sel;
        selA = blk == rb2 ? sel : z4; selB = blk == rb2 ? z4 : sel;
        PIN(wA0); PIN(wA1); PIN(sel0); PIN(selA); PIN(selB);
      }
      f32x16 aq[2] = {zero16, zero16}, ak[2] = {zero16, zero16}, av[2] = {zero16, zero16};
      f32x4 cs3[3] = {{0, 0, 0, 0}, {0, 0, 0, 0}, {0, 0, 0, 0}};
      const u32 one2 = 0x3f803f80u, csel1 = l15 == blk ? one2 : 0u;
      const u32x4 csel = {csel1, csel1, csel1, csel1};
      const u32 xb0 = (64 * rb2 + l31) * 128 + ((hh ^ ((l31 >> 1) & 7)) << 4);
      const u32 wb0 = 16384 + (8 * hh + (l15 >> 2)) * 128 + ((hp ^ (l15 >> 3)) << 6) + blk * 32 + (l15 & 3) * 8;
      u32x4 xa[2][2] = {{z4, z4}, {z4, z4}}, wa[2][3] = {{z4, z4, z4}, {z4, z4, z4}};
      u32 xb, wb, slot = 0;
      auto a_step = [&](int kc, bool rd, int kr, bool cs) {
#pragma unroll
        for (int q = 0; q < 6; ++q) {
          const int sec = q >> 1, a = q & 1;
          if (rd) {
            if (q < 2) xa[kr & 1][q] = ldsr128((xb ^ (kr * 32)) + q * 4096);
            if (q >= 2 && q < 5) { const u32 ad = wb + (q - 2) * 8192 + kr * 2048; wa[kr & 1][q - 2] = cat(trd(ad), trd(ad + 512)); }
          }
          SB();
          if (sec == 0) { aq[a] = mfma(wa[kc & 1][0], xa[kc & 1][a], aq[a]); PIN(aq[a]); }
          if (sec == 1) { ak[a] = mfma(wa[kc & 1][1], xa[kc & 1][a], ak[a]); PIN(ak[a]); }
          if (sec == 2) { av[a] = mfma(xa[kc & 1][a], wa[kc & 1][2], av[a]); PIN(av[a]); }
          SB();
        }
        if (cs && rb2 == (kc & 1)) {
#pragma unroll
          for (int q = 0; q < 3; ++q) { cs3[q] = mfma16(csel, wa[kc & 1][q], cs3[q]); PIN(cs3[q]); }
        }
      };
      ST(13);
      auto a_chunk = [&](bool cs0, bool cs) {
        BARRIER();
        xb = opv(slot + xb0); wb = opv(slot + wb0);
        slot = slot == 81920 ? 0 : slot + 40960;
        a_step(3, true, 0, cs0); a_step(0, true, 1, cs); a_step(1, true, 2, cs); a_step(2, true, 3, cs);
      };
#pragma unroll 1
      for (int c = 0; c < 2; ++c) a_chunk(true, true);
      ST(1); ST(56); CT(57);
      if (rb2 == 1) {
#pragma unroll
        for (int q = 0; q < 3; ++q) cs3[q] = mfma16(csel, wa[1][q], cs3[q]);
      }
      if (lane < 16) {
        const u32 ca = L_ACP + hp * 384 + lane * 4;
#pragma unroll
        for (int q = 0; q < 3; ++q) { lds_addf(ca + q * 128, cs3[q][0]); lds_addf(ca + q * 128 + 64, cs3[q][1]); }
      }
#pragma unroll 1
      for (int c = 2; c < 8; ++c) {
        a_chunk(false, false);
        if (w == 3) {
          if (c == 2 && lane < 48) st128c(rws, WS_CS + (a_rb * 8 + a_grp) * 768 + lane * 16, ldsr128(L_ACP + lane * 16));
          if (c == 3) { VMCNT(0); flag_set<1>(ws, WS_FC, a_rb * 8 + a_grp, tag, lane); }
        }
      }
      a_step(3, false, 0, false);
      ST(2); ST(58); CT(59);
      BARRIER();
      const u32 stb = opv(L_AST + (64 * rb2 + 4 * hh) * 4), stl = opv(L_AST + (64 * rb2 + l31) * 4);
      float sm[2], sr[2];
#pragma unroll
      for (int a = 0; a < 2; ++a) { sm[a] = *(LAS float*)(stl + a * 128); sr[a] = *(LAS float*)(stl + 512 + a * 128) * 1.4426950408889634f; }
      auto feat = [&](const f32x16& acc, float m, const f32x4 (&c4)[4]) {
        u32 t[8];
        sfor<0, 8>([&](auto ic) { constexpr int k = decltype(ic)::v;
          t[k] = pk2v(pfma(sp2(-m), f32x2{c4[k >> 1][2 * (k & 1)], c4[k >> 1][2 * (k & 1) + 1]}, g2<k>(acc))); });
        return mfma(wA1, u32x4{t[4], t[5], t[6], t[7]}, mfma(wA0, u32x4{t[0], t[1], t[2], t[3]}, zero16));
      };
      const u32 csb = opv(L_ACS + hp * 384 + hh * 16);
      const u32 rec = (a_grp * 2 + hp) * 2 + rb2;
      {
        f32x4 ck[4];
#pragma unroll
        for (int i = 0; i < 4; ++i) ck[i] = ldsf4(csb + 128 + 32 * i);
        const float cv = *(LAS float*)(L_ACS + hp * 384 + 256 + l31 * 4);
        f32x16 pk[2];
#pragma unroll
        for (int a = 0; a < 2; ++a) pk[a] = feat(ak[a], sm[a], ck);
        f32x16 kv[2] = {zero16, zero16};
#pragma unroll
        for (int a = 0; a < 2; ++a) {
          f32x4 m4[4], r4[4];
#pragma unroll
          for (int i = 0; i < 4; ++i) { m4[i] = ldsf4(stb + a * 128 + 32 * i); r4[i] = ldsf4(stb + a * 128 + 512 + 32 * i); }
          u32x4 pos[2], neg[2];
          hedgehog(pk[a], sr[a], pos, neg);
          f32x16 t[2];
#pragma unroll
          for (int e = 0; e < 2; ++e) t[e] = mfma(neg[e], selB, mfma(pos[e], selA, zero16));
          u32 vb[8];
          sfor<0, 8>([&](auto ic) { constexpr int k = decltype(ic)::v;
            vb[k] = pk2v(pfma(f32x2{m4[k >> 1][2 * (k & 1)], m4[k >> 1][2 * (k & 1) + 1]}, sp2(-cv), g2<k>(av[a])) * f32x2{r4[k >> 1][2 * (k & 1)], r4[k >> 1][2 * (k & 1) + 1]}); });
#pragma unroll
          for (int e = 0; e < 2; ++e) {
            const bool own = blk == e;
            const u32x4 v0 = {own ? vb[0] : one2, own ? vb[1] : one2, own ? vb[2] : one2, own ? vb[3] : one2};
            const u32x4 v1 = {own ? vb[4] : one2, own ? vb[5] : one2, own ? vb[6] : one2, own ? vb[7] : one2};
            kv[e] = mfma(pk8<8>(t[e]), v1, mfma(pk8<0>(t[e]), v0, kv[e]));
          }
        }
        const u32 kab = L_AKV + w * 4352;
#pragma unroll
        for (int e = 0; e < 2; ++e) {
          if (blk == e || l15 == 0) {
            const u32 ka = opv(kab + (blk == e ? lane : 64 + e * 2 + hh) * 16);
            ldsw128(ka, get4<0>(kv[e])); ldsw128(ka + 1088, get4<4>(kv[e])); ldsw128(ka + 2176, get4<8>(kv[e])); ldsw128(ka + 3264, get4<12>(kv[e]));
          }
        }
        cw_st(L_SYN + w * 4, 1);
        const u32 zo = 1024 + ((l31 >> 2) & 1) * 1088 + (l31 >> 3) * 16 + (l31 & 3) * 4, kp = L_AKV + (w ^ 1) * 4352 + 2176;
        const f32x4 o0 = ldsf4(kab + lane * 16), o1 = ldsf4(kab + 1088 + lane * 16);
        const float oz = *(LAS float*)(kab + zo);
        ST(3);
        lds_wait<false>(L_SYN + (w ^ 1) * 4, 1);
        const f32x4 s0 = o0 + ldsf4(kp + lane * 16), s1 = o1 + ldsf4(kp + 1088 + lane * 16);
        const float zz = oz + *(LAS float*)(kp + zo);
        st128c(rws, WS_KV + (a_rb * 32 + rec) * 1024 + lane * 16, u32x4{pk2(s0[0], s0[1]), pk2(s0[2], s0[3]), pk2(s1[0], s1[1]), pk2(s1[2], s1[3])});
        if (lane < 32) __builtin_amdgcn_raw_buffer_store_b32(__float_as_uint(zz), rws, WS_Z + (a_rb * 32 + rec) * 128 + lane * 4, 0, 16);
      }
      u32x4 qp[2][2], qn[2][2];
      {
        f32x4 cq[4];
#pragma unroll
        for (int i = 0; i < 4; ++i) cq[i] = ldsf4(csb + 32 * i);
        const f32x16 p0 = feat(aq[0], sm[0], cq), p1 = feat(aq[1], sm[1], cq);
        publish(ws, WS_FKV, a_rb * 8 + a_grp, tag, L_CNT, 4, lane);
        ST(4);
        hedgehog(p0, sr[0], qp[0], qn[0]);
        hedgehog(p1, sr[1], qp[1], qn[1]);
#pragma unroll
        for (int a = 0; a < 2; ++a) { PIN(qp[a][0]); PIN(qp[a][1]); PIN(qn[a][0]); PIN(qn[a][1]); }
      }
      ST(5);
      if (w == 0) { flag_wait<1>(ws, WS_FKV, xc, l15 * 8 + a_grp, 0, tag); cw_st(L_LND, 1); }
      else lds_wait<false>(L_LND, 1);
      ST(6);
      {
        u32x4 kvl[16]; u32 zl[16];
#pragma unroll
        for (int q = 0; q < 16; ++q) {
          kvl[q] = ld128c(rws, WS_KV + lane * 16, (q * 32 + rec) * 1024);
          zl[q] = __builtin_amdgcn_raw_buffer_load_b32(rws, WS_Z + l31 * 4, (q * 32 + rec) * 128, 16);
        }
        SB();
        f32x16 tot = zero16;
        float zz = 0.f;
#pragma unroll
        for (int q = 0; q < 16; ++q) { tot = mfma(sel0, kvl[q], tot); zz += __uint_as_float(zl[q]); }
        ldsw128(L_AT + w * 1024 + lane * 16, pk8<0>(tot));
        if (lane < 32) *(LAS u16*)(L_AZT + w * 64 + lane * 2) = (u16)pk2(zz, zz);
      }
      cw_st(L_SYN + w * 4, 2);
      lds_wait<false>(L_SYN + (w ^ 1) * 4, 2);
      ST(14);
      {
        const u32x4 k0 = ldsr128(L_AT + 2 * hp * 1024 + lane * 16), k1 = ldsr128(L_AT + (2 * hp + 1) * 1024 + lane * 16);
        const u32x4 y0 = ldsr128(L_AZT + 2 * hp * 64 + ((1 - blk) * 2 + hh) * 16), y1 = ldsr128(L_AZT + (2 * hp + 1) * 64 + ((1 - blk) * 2 + hh) * 16);
#pragma unroll
        for (int e = 0; e < 2; ++e) {
          const bool own = blk == e;
          const u32x4 a0 = own ? k0 : y0, a1 = own ? k1 : y1;
#pragma unroll
          for (int a = 0; a < 2; ++a) {
            const f32x16 nd = mfma(a1, qn[a][e], mfma(a0, qp[a][e], zero16));
            const float rd = __builtin_amdgcn_rcpf(nd[8 - 8 * e] + 1e-6f);
            const int o = 8 * e;
            st128c(rws, WS_O + ((a_grp * 4 + 2 * hp + e) * 64 + a_rb * 4 + 2 * rb2 + a) * 1024 + lane * 16,
                   u32x4{pk2(nd[o] * rd, nd[o + 1] * rd), pk2(nd[o + 2] * rd, nd[o + 3] * rd), pk2(nd[o + 4] * rd, nd[o + 5] * rd), pk2(nd[o + 6] * rd, nd[o + 7] * rd)});
          }
        }
        cw_st(L_PROG + w * 4, 64);
        publish(ws, WS_FO, a_rb * 8 + a_grp, tag, L_CNT + 4, 4, lane);
      }
      ST(7);
    }
  } else {
    const int rot = 8 * (b_dt + (j >> 2));
    if (isG) {
      const int li = xc * 16 + j;
      const int rl = 4 * w + (lane >> 4), row = li * 16 + rl;
      u32x4 xr[4];
#pragma unroll
      for (int n = 0; n < 4; ++n) xr[n] = __builtin_amdgcn_raw_buffer_load_b128(rx, row * 1024 + l15 * 16 + n * 256, 0, 0);
      float s1[2] = {0.f, 0.f}, s2[2] = {0.f, 0.f};
#pragma unroll
      for (int i = 0; i < 16; ++i) { const u32 v = xr[i >> 2][i & 3]; s1[i & 1] = dot2(v, 0x3f803f80u, s1[i & 1]); s2[i & 1] = dot2(v, v, s2[i & 1]); }
      ST(1);
      const float m = sum16(s1[0] + s1[1]) * (1.f / D);
      const float var = fmaxf(sum16(s2[0] + s2[1]) * (1.f / D) - m * m, 0.f);
      const float mb = bfr(m);
      const float ri = __builtin_amdgcn_rcpf(bfr(__builtin_amdgcn_sqrtf(bfr(bfr(var) + 1e-5f))));
      const float m2 = bfr((m - mb) * ri);
      const float r2 = __builtin_amdgcn_rcpf(bfr(__builtin_amdgcn_sqrtf(bfr(bfr(var * ri * ri) + 1e-5f))));
      const float ha = ri * r2, hb = -(mb * ri + m2) * r2;
      if (l15 == 0) *(LAS u32x2*)(L_LNS + rl * 8) = u32x2{__float_as_uint(mb), __float_as_uint(ri)};
      const u32 wa = opv(L_LN + rl * LN_PITCH + l15 * 16);
#pragma unroll
      for (int n = 0; n < 4; ++n) {
        u32x4 hv;
#pragma unroll
        for (int i = 0; i < 4; ++i) hv[i] = pk2v(pfma(unpk(xr[n][i]), sp2(ha), sp2(hb)));
        ldsw128(wa + n * 256, hv);
      }
      cw_st(L_SYN + w * 4, 1);
      ST(14);
      const u32 ad = opv(L_LN + (8 * hh + (l15 >> 2)) * LN_PITCH + w * 256 + blk * 32 + (l15 & 3) * 8);
      const u32 ho = WS_H + (li * 16 + 4 * w) * 1024 + lane * 16;
      lds_wait<false>(L_SYN + (lane & 3) * 4, 1);
#pragma unroll
      for (int rr = 0; rr < 4; ++rr) st128c(rws, ho + rr * 1024, cat(trd(ad + rr * 64), trd(ad + rr * 64 + 4 * LN_PITCH)));
      if (w == 0 && lane < 8) st128c(rws, WS_STAT + li * 128 + lane * 16, ldsr128(L_LNS + lane * 16));
      cw_st(L_SYN + w * 4, 2);
      ST(2);
      publish<1>(ws, WS_FH, li, tag, L_CNT + 12, 4, lane);
      ST(3);
      if (roleB) {
        u32x2 bias[6];
#pragma unroll
        for (int t = 0; t < 6; ++t) { int f = b_ft * 96 + (t >> 1) * 32 + 16 * b_kz + 8 * (t & 1) + 4 * hh; if (f > F - 4) f = F - 4; bias[t] = *(const GAS u32x2*)(b1 + f); }
        f32x16 acc[3];
#pragma unroll
        for (int t = 0; t < 3; ++t) {
          acc[t] = zero16;
#pragma unroll
          for (int i = 0; i < 2; ++i) { acc[t][4 * i] = lo16(bias[2 * t + i].x); acc[t][4 * i + 1] = hi16(bias[2 * t + i].x); acc[t][4 * i + 2] = lo16(bias[2 * t + i].y); acc[t][4 * i + 3] = hi16(bias[2 * t + i].y); }
          PIN(acc[t]);
        }
        cw_st(L_PROG + w * 4, 32);
        if (w == 0) { flag_wait<1>(ws, WS_FU, xc, ID_HALL, 0, tag); cw_st(L_GO, 1); }
        else wait_word(L_GO, 1);
        ST(4); ST(56); CT(57);
        const u32 hv0 = WS_H + b_kz * (32 * 32768) + (b_dt * 4 + w) * 1024 + lane * 16, hv1 = hv0 + 16384;
        u32x4 hr[16];
        u32 hso = (rot & 31) * 32768;
        auto h_next = [&]() { hso = (hso + 32768) & (32 * 32768 - 1); };
#pragma unroll
        for (int i = 0; i < 8; ++i) { hr[2 * i] = ld128c(rws, hv0, hso); hr[2 * i + 1] = ld128c(rws, hv1, hso); h_next(); }
        SB();
        const u32 b0 = (8 * hh + (l15 >> 2)) * 192 + (blk ^ b_kz) * 32 + (l15 & 3) * 8;
        u32x4 wfr[2][6];
        u32 pos = 2, gb = opv(b0 + 2 * 24576);
        bool ready = true;
        auto b_rd = [&](int buf, int k, int t) {
          const u32 ad = gb + k * 6144 + (t / 3) * 3072 + (t % 3) * 64;
          wfr[buf][t] = cat(trd(ad), trd(ad + 768));
        };
        lds_wait<false>(aFULL, 9);
#pragma unroll
        for (int t = 0; t < 6; ++t) b_rd(0, 0, t);
        u32 progv = 32, need = 9;
        const u32 proga = L_PROG + w * 4;
        auto b_pair = [&](bool more) {
#pragma unroll
          for (int mi = 0; mi < 2; ++mi) {
            ST2R(7 + need);
            u32 fv, gbn;
#pragma unroll
            for (int k = 0; k < 4; ++k) {
              if (k == 3) {
                gb = gbn;
                if ((more || mi < 1) && __builtin_expect(!ready, 0)) lds_wait<false>(aFULL, need);
              }
#pragma unroll
              for (int t = 0; t < 6; ++t) {
                if (more || mi < 1 || k < 3) b_rd((k + 1) & 1, (k + 1) & 3, t);
                if (k == 2 && t == 1) fv = *(volatile LAS u32*)aFULL;
                if (k == 2 && t == 5) cw_st(proga, progv);
                SB();
                acc[t % 3] = mfma(wfr[k & 1][t], hr[(mi * 4 + k) * 2 + t / 3], acc[t % 3]);
                PIN(acc[t % 3]);
                SB();
                if (t == 2 && more) hr[(mi * 4 + k) * 2] = ld128c(rws, hv0, hso);
                if (t == 5 && more) { hr[(mi * 4 + k) * 2 + 1] = ld128c(rws, hv1, hso); h_next(); }
                if (k == 1 && t == 1) { progv += 4; PIN(progv); pos = pos == 5 ? 0 : pos + 1; ++need; }
                if (k == 1 && t == 3) gbn = opv(b0 + pos * 24576);
                if (k == 2 && t == 5) ready = __ballot(fv < need) == 0;
                SB();
              }
            }
          }
        };
#pragma unroll 1
        for (int mo = 0; mo < 3; ++mo) b_pair(true);
        b_pair(false);
        ST(5); ST(58); CT(59);
        const u32 pa = WS_P + w * 6144 + lane * 16;
        u32x4 oth[6];
#pragma unroll
        for (int t = 0; t < 3; ++t) {
          st128c(rws, pa + b_id * 24576 + 2 * t * 1024, get4<8>(acc[t]));
          st128c(rws, pa + b_id * 24576 + (2 * t + 1) * 1024, get4<12>(acc[t]));
        }
        publish<1>(ws, WS_FP, b_id, tag, L_CNT + 4, 4, lane);
        ST(30);
        if (w == 0) { flag_wait<1>(ws, WS_FP, 0, b_id ^ 1, 0, tag); cw_st(L_LND, 1); }
        else lds_wait<false>(L_LND, 1);
        ST(31);
#pragma unroll
        for (int t = 0; t < 6; ++t) oth[t] = ld128c(rws, pa + (b_id ^ 1) * 24576 + t * 1024, 0);
        SB();
#pragma unroll
        for (int t = 0; t < 3; ++t) {
          u32x4 uv;
#pragma unroll
          for (int i = 0; i < 4; ++i) {
            const f32x2 v = f32x2{acc[t][2 * i], acc[t][2 * i + 1]} + f32x2{__uint_as_float(oth[2 * t + (i >> 1)][2 * (i & 1)]), __uint_as_float(oth[2 * t + (i >> 1)][2 * (i & 1) + 1])};
            const float c0 = -2.f * 0.7978845608028654f * 1.4426950408889634f;
            const f32x2 zz = v * pfma(v * v, sp2(0.044715f * c0), sp2(c0));
            uv[i] = pk2v(v * rc2(ex2(zz) + sp2(1.f)));
          }
          if (b_ft * 6 + 2 * t < 86) st128c(rws, WS_U + ((b_ft * 6 + 2 * t + b_kz) * 16 + b_dt * 4 + w) * 1024 + lane * 16, uv);
        }
        cw_st(L_SYN + w * 4, 3);
        ST(6);
        publish(ws, WS_FU, b_id, tag, L_CNT, 4, lane);
        ST(7);
      } else {
        if (bid == 8 * 31 + 7 && w == 0) {
          flag_wait<2>(ws, WS_FH, 0, lane, lane + 64, tag);
          flag_set(ws, WS_FU, ID_HALL, tag, lane);
        }
        cw_st(L_PROG + w * 4, 64);
      }
    } else if (roleB) {
      const rsrc_t rw1 = mkrs(w1, S * F * 2);
      u32 vo[6];
      {
        u32 rr = (lane * 43) >> 9, ch = lane - rr * 12;
        const u32 cm = b_ft == 14 ? 3 : 15;
#pragma unroll
        for (int n = 0; n < 6; ++n) {
          vo[n] = rr * (F * 2) + (ch & cm) * 16 + b_ft * 192;
          ch += 4; rr += 5;
          if (ch >= 12) { ch -= 12; ++rr; }
        }
      }
      u32 pos = 2;
      auto b_issue = [&](int m) {
        const u32 so = (b_kz * 32 + ((4 * m + hw + rot) & 31)) * (32 * F * 2);
#pragma unroll
        for (int n = 0; n < 6; ++n) glds(rw1, vo[n], so, pos * 24576 + hw * 6144 + n * 1024);
      };
      if (BPRE) b_issue(8);
      ST(13);
      wait_word(L_CNT + 12, 4);
      if (!BPRE) b_issue(8);
      ST(16);
#pragma unroll 1
      for (int m = 9; m < 16; ++m) {
        pos = pos == 5 ? 0 : pos + 1;
        if (m >= 14) { VMCNT(0); pub(m); lds_wait<true>(aPROG, 4 * (m - 5)); }
        b_issue(m);
        VMCNT(18);
        pub(m - 2);
        if (m == 13) ST(17);
      }
      ST(18);
      VMCNT(12); pub(14); VMCNT(6); pub(15); VMCNT(0); pub(16);
      ST(4);
    }
  }

  if (isG) {
    const int p = w & 1, kh = w >> 1;
    if (w == 0) { flag_wait<1>(ws, WS_FU, xc, ((l31 < 30 ? l31 >> 1 : 0) * 4 + (dtile >> 1)) * 2 + (lane & 1), 0, tag); cw_st(L_GO, 2); }
    else wait_word(L_GO, 2);
    ST(8); ST(60); CT(61);
    const u32 uv0 = WS_U + (dtile * 2 + p) * 1024 + lane * 16, uv1 = uv0 + 16384, ov0 = WS_O + (stile * 2 + p) * 1024 + lane * 16, ov1 = ov0 + 65536;
    u32x4 ar[16];
    u32 uso = (kh + crot) * 32768, oso = ((kh + orot) & 15) * 131072;
    auto a_load = [&](int i, int h) {
      if (i < 22) {
        ar[(2 * i + h) & 15] = ld128c(rws, h ? uv1 : uv0, uso);
        if (h) { uso += 65536; if (uso >= 43 * 32768) uso -= 43 * 32768; }
      } else {
        ar[(2 * i + h) & 15] = ld128c(rws, h ? ov1 : ov0, oso);
        if (h) oso = (oso + 262144) & (16 * 131072 - 1);
      }
    };
#pragma unroll
    for (int i = 0; i < 8; ++i) { a_load(i, 0); a_load(i, 1); }
    SB();
    f32x16 ac[2] = {zero16, zero16};
    u32 tb[2], tg[2];
#pragma unroll
    for (int t = 0; t < 2; ++t) tb[t] = (4 * hh + (l15 >> 2)) * 128 + ((t ^ kh ^ (l15 >> 3)) << 6) + blk * 32 + (l15 & 3) * 8 + kh * 4096;
    u32x4 wfr[2][4];
    auto c_rd = [&](auto ic, int t) {
      constexpr int i = decltype(ic)::v;
      if (i >= 30) return;
      const u32 ad = tg[t & 1] + (i & 1) * 8192 + (t >> 1) * 2048;
      wfr[i & 1][t] = cat(trd(ad), trd(ad + 1024));
    };
    lds_wait<false>(aFULL, 17);
    tg[0] = opv(tb[0] + 6 * 16384); tg[1] = opv(tb[1] + 6 * 16384);
#pragma unroll
    for (int t = 0; t < 4; ++t) c_rd(IC<0>{}, t);
    u32 fv, tn[2], progv = 64;
    bool ready = true;
    const u32 proga = L_PROG + w * 4, ya = opv(L_CY + w * 8192 + lane * 16);
    sfor<0, 30>([&](auto ic) {
      constexpr int i = decltype(ic)::v;
      constexpr bool ld = i + 8 < 30;
      if ((i & 1) == 0) ST2(32 + i / 2);
      if (i == 14) { ST(14); wait_word(L_GOO, 1); ST(9); }
      if (i == 21 && kh == 1) { ar[10] = u32x4{0, 0, 0, 0}; ar[11] = ar[10]; }
      if (i == 22) {
        lds_wait<false>(aPROG, 64 + 36);
#pragma unroll
        for (int t = 0; t < 2; ++t) {
          ldsw128(ya + t * 4096, get4<0>(ac[t])); ldsw128(ya + t * 4096 + 1024, get4<4>(ac[t]));
          ldsw128(ya + t * 4096 + 2048, get4<8>(ac[t])); ldsw128(ya + t * 4096 + 3072, get4<12>(ac[t]));
        }
        cw_st(L_SYN + w * 4, 6);
        ac[0] = zero16; ac[1] = zero16;
      }
      if ((i & 1) && i < 29) {
        tg[0] = tn[0]; tg[1] = tn[1];
        if (__builtin_expect(!ready, 0)) lds_wait<false>(aFULL, 16 + i / 2 + 2);
      }
#pragma unroll
      for (int t = 0; t < 4; ++t) {
        c_rd(IC<i + 1>{}, t);
        if (!(i & 1) && t == 0 && i < 28) fv = *(volatile LAS u32*)aFULL;
        if (!(i & 1) && t == 3) cw_st(proga, progv);
        SB();
        if (i < 22) ac[t & 1] = mfma(ar[(2 * i + (t >> 1)) & 15], wfr[i & 1][t], ac[t & 1]);
        else ac[t & 1] = mfma(wfr[i & 1][t], ar[(2 * i + (t >> 1)) & 15], ac[t & 1]);
        PIN(ac[t & 1]);
        SB();
        if (ld && (t & 1)) a_load(i + 8, t >> 1);
        if (!(i & 1) && t == 0) { progv += 4; PIN(progv); tn[0] = opv(tb[0] + ((7 + i / 2) % 9) * 16384); tn[1] = opv(tb[1] + ((7 + i / 2) % 9) * 16384); }
        if (!(i & 1) && t == 3 && i < 28) ready = __ballot(fv < 16 + i / 2 + 2) == 0;
        SB();
      }
    });
    ST(10); ST(62); CT(63);
    lds_wait<false>(aPROG, 64 + 40);
    const u32 sa = opv(L_CS + w * 4096 + lane * 16);
    ldsw128(sa, pk8<0>(ac[0])); ldsw128(sa + 1024, pk8<8>(ac[0])); ldsw128(sa + 2048, pk8<0>(ac[1])); ldsw128(sa + 3072, pk8<8>(ac[1]));
    cw_st(L_SYN + w * 4, 7);
  } else {
    u32x4 rc[6][4];
    if (!roleA) lds_wait<true>(aPROG, CHOLD);
#pragma unroll
    for (int n = 0; n < 6; ++n) {
      if (n > 2 && !roleA) lds_wait<true>(aPROG, c_need(n));
      c_issue(n);
      ST2(32 + n);
    }
#pragma unroll
    for (int q = 9; q < 15; ++q) {
      rsrc_t rw; u32 vo2, so2, st2;
      c_src(q, rw, vo2, so2, st2);
      if (q == RCUT && roleA) lds_wait<true>(aPROG, 64);
      if (q == BCUT && roleB) { if (BMODE) lds_wait<true>(L_SYN + (lane & 3) * 4, 3); else wait_word(L_CNT, 4); }
#pragma unroll
      for (int m = 0; m < 4; ++m) rc[q - 9][m] = __builtin_amdgcn_raw_buffer_load_b128(rw, vo2, so2 + m * st2, 0);
    }
    SB();
    ST(3);
    VMCNT(24); pub(16 + 6);
    lds_wait<true>(aPROG, 64);
    if (roleB) wait_word(L_CNT + 4, 4);
    c_issue(6); c_issue(7); c_issue(8);
    const int srow = stile * 64 + (hw >> 1) * 32 + l31;
    u32x2 xin[4];
#pragma unroll
    for (int t = 0; t < 4; ++t) xin[t] = *(const GAS u32x2*)(x + srow * D + dtile * 64 + (hw & 1) * 32 + 8 * t + 4 * hh);
    const u32 b2v = b2[srow];
    SB();
    VMCNT(5); pub(16 + 9);
    ST(8);
    if (w == 4) {
      flag_wait<1>(ws, WS_FO, xc, (stile >> 1) * 8 + (lane & 7), 0, tag);
      cw_st(L_GOO, 1);
    }
    ST(9);
    const u32 wa = hw * 4096 + lane * 16;
#pragma unroll
    for (int n = 9; n < 15; ++n) {
      lds_wait<true>(aPROG, c_need(n));
      const u32 wb = opv(wa + ((6 + n) % 9) * 16384);
#pragma unroll
      for (int m = 0; m < 4; ++m) ldsw128(wb + m * 1024, rc[n - 9][m]);
      pub(16 + n + 1);
      ST2(32 + n);
    }
    const int p = hw & 1, kh = hw >> 1;
    f32x2 xf[8];
#pragma unroll
    for (int q = 0; q < 8; ++q) { xf[q] = unpk(xin[q >> 1][q & 1]); PIN(xf[q]); }
    const u32 y0 = opv(L_CY + hw * 8192 + lane * 16), y1 = opv(L_CY + (hw ^ 2) * 8192 + 4096 + lane * 16);
    const u32 s0 = opv(L_CS + kh * 4096 + p * 2048 + lane * 16), s1 = opv(L_CS + (kh + 2) * 4096 + (1 - p) * 2048 + lane * 16);
    u16* const op = out + srow * D + dtile * 64 + p * 32 + 4 * hh;
    lds_wait<true>(aPROG, 64 + 40);
    __builtin_amdgcn_s_setprio(1);
    lds_wait<false>(L_SYN + (p + 2 * (lane & 1)) * 4, 6);
    f32x2 gg[8];
    {
      f32x4 ya4[4], yb4[4];
#pragma unroll
      for (int t = 0; t < 4; ++t) { ya4[t] = ldsf4(y0 + t * 1024); yb4[t] = ldsf4(y1 + t * 1024); }
      SB();
#pragma unroll
      for (int t = 0; t < 4; ++t) {
        const f32x4 y4 = ya4[t] + yb4[t];
#pragma unroll
        for (int q = 0; q < 2; ++q) {
          const f32x2 yy = f32x2{y4[2 * q], y4[2 * q + 1]} + sp2(lo16(b2v));
          gg[2 * t + q] = yy * rc2(ex2(yy * sp2(-1.4426950408889634f)) + sp2(1.f));
          PIN(gg[2 * t + q]);
        }
      }
    }
    ST(15);
    lds_wait<false>(L_SYN + (kh + 2 * (lane & 1)) * 4, 7);
    const u32x4 c0 = ldsr128(s0), c1 = ldsr128(s0 + 1024), d0 = ldsr128(s1), d1 = ldsr128(s1 + 1024);
#pragma unroll
    for (int t = 0; t < 4; ++t) {
      u32x2 ov;
#pragma unroll
      for (int i = 0; i < 2; ++i) {
        const int q = 2 * t + i;
        ov[i] = pk2v(pfma(unpk(q < 4 ? c0[q & 3] : c1[q & 3]) + unpk(q < 4 ? d0[q & 3] : d1[q & 3]), gg[q], xf[q]));
      }
      *(GAS u32x2*)(op + 8 * t) = ov;
    }
    if (w == 4 && bid == 0) {
      if (lane == 0) __hip_atomic_store((GAS u64*)(ws + WS_EPOCH), tag, __ATOMIC_RELAXED, __HIP_MEMORY_SCOPE_AGENT);
    }
  }
#endif
}
#undef tag
}

extern "C" void kernel_launch(void* const* d_in, const int* in_sizes, int n_in, void* d_out, int out_size, void* d_ws, size_t ws_size,
                              hipStream_t stream) {
  using namespace hk;
  (void)hipFuncSetAttribute((const void*)fused, hipFuncAttributeMaxDynamicSharedMemorySize, LDS_BYTES);
  fused<<<256, 512, LDS_BYTES, stream>>>((const u16*)d_in[0], (const u16*)d_in[1], (const u16*)d_in[2], (const u16*)d_in[3], (const u16*)d_in[5],
                                         (const u16*)d_in[6], (const u16*)d_in[7], (const u16*)d_in[8], (u16*)d_out, (char*)d_ws);
}
```
